```python
import torch

def _softmax(x, axis=-1):
    return torch.softmax(x, dim=axis)
def _gelu(x):
    return torch.nn.functional.gelu(x, approximate="tanh")
def _silu(x):
    return torch.nn.functional.silu(x)
def _sigmoid(x):
    return torch.sigmoid(x)
def _layernorm(x, axis=-1, eps=1e-5):
    m = x.mean(dim=axis, keepdim=True)
    v = x.var(dim=axis, keepdim=True, unbiased=False)
    return (x - m) / torch.sqrt(v + eps)
def _scatter_add(tgt, idx, src, axis=0):
    return tgt.clone().index_add_(axis, idx, src)
def _segment_sum(src, seg, n):
    out = torch.zeros((n,) + src.shape[1:], dtype=src.dtype)
    return out.index_add_(0, seg, src)
def _pad(x, pw):
    flat = tuple(v for pair in reversed(pw) for v in pair)
    return torch.nn.functional.pad(x, flat)

kernel_name = 'interleave_layers__linear_attn_block__geglu_ffn__prompt'

def setup_inputs():

    _w = (lambda a: (lambda t: t.to(torch.bfloat16) if t.is_floating_point() else t)(torch.as_tensor(a)))
    def _A_setup():
        B, S, D = 1, 2048, 576
        return {
            "x": _w((lambda *s: torch.randn(*s, dtype=torch.bfloat16) * 0.05)(B, S, D)),
            "wqkv": _w((lambda *s: torch.randn(*s, dtype=torch.bfloat16) * 0.05)(D, 3 * D)),
            "wo": _w((lambda *s: torch.randn(*s, dtype=torch.bfloat16) * 0.05)(D, D)),
        }
    _A_in = _A_setup()

    def _B_setup():
        B, S, D, F = 1, 2048, 576, 4608
        return {
            "x": _w((lambda *s: torch.randn(*s, dtype=torch.bfloat16) * 0.05)(B, S, D)),
            "w_gate": _w((lambda *s: torch.randn(*s, dtype=torch.bfloat16) * 0.05)(D, F)),
            "w_up": _w((lambda *s: torch.randn(*s, dtype=torch.bfloat16) * 0.05)(D, F)),
            "w_down": _w((lambda *s: torch.randn(*s, dtype=torch.bfloat16) * 0.05)(F, D)),
        }
    _B_in = _B_setup()

    return {
        "A_x": _A_in["x"],
        "A_wqkv": _A_in["wqkv"],
        "A_wo": _A_in["wo"],
        "B_x": _B_in["x"],
        "B_w_gate": _B_in["w_gate"],
        "B_w_up": _B_in["w_up"],
        "B_w_down": _B_in["w_down"],
    }

def reference(A_x, A_wqkv, A_wo, B_x, B_w_gate, B_w_up, B_w_down):
    def _A_ref(x, wqkv, wo):
        B, S, D = x.shape
        H = 32; Dh = D // H
        qkv = (lambda x, *d: x.permute(*d))((x @ wqkv).reshape(B, S, 3, H, Dh), 2, 0, 3, 1, 4)
        q, k, v = qkv[0], qkv[1], qkv[2]
        phi = lambda t: torch.where(t > 0, t + 1, torch.exp(t)) + 1e-6
        pq, pk = phi(q), phi(k)
        kv = torch.einsum("bhsd,bhse->bhde", pk, v)
        z = torch.einsum("bhsd->bhd", pk)
        num = torch.einsum("bhqd,bhde->bhqe", pq, kv)
        den = torch.einsum("bhqd,bhd->bhq", pq, z)[..., None] + 1e-6
        o = num / den
        return (lambda x, *d: x.permute(*d))(o, 0, 2, 1, 3).reshape(B, S, D) @ wo

    def _B_ref(x, w_gate, w_up, w_down):
        return (_gelu(x @ w_gate) * (x @ w_up)) @ w_down

    x = A_x
    x = x + _A_ref(_layernorm(x, axis=-1), A_wqkv, A_wo)
    x = x + _B_ref(_layernorm(x, axis=-1), B_w_gate, B_w_up, B_w_down)
    x = x + _A_ref(_layernorm(x, axis=-1), A_wqkv, A_wo)
    x = x + _B_ref(_layernorm(x, axis=-1), B_w_gate, B_w_up, B_w_down)
    return x
```

```cpp
#include <hip/hip_runtime.h>
#include <hip/hip_bf16.h>
#include <stdint.h>
typedef __hip_bfloat16 bf16;
#define S 2048
#define D 576
#define F 4608
#define H 32
#define DH 18

__device__ __forceinline__ float b2f(bf16 x){ return __bfloat162float(x);}
__device__ __forceinline__ bf16 f2b(float x){ return __float2bfloat16(x);}

__global__ void ln_k(const float* x, bf16* y){
  int r = blockIdx.x; __shared__ float red[256];
  float s=0; for(int i=threadIdx.x;i<D;i+=256) s+=x[r*D+i];
  red[threadIdx.x]=s; __syncthreads();
  for(int o=128;o>0;o>>=1){ if(threadIdx.x<o) red[threadIdx.x]+=red[threadIdx.x+o]; __syncthreads(); }
  float m=red[0]/D; __syncthreads();
  s=0; for(int i=threadIdx.x;i<D;i+=256){ float d=x[r*D+i]-m; s+=d*d; }
  red[threadIdx.x]=s; __syncthreads();
  for(int o=128;o>0;o>>=1){ if(threadIdx.x<o) red[threadIdx.x]+=red[threadIdx.x+o]; __syncthreads(); }
  float v=red[0]/D; float rs=1.0f/sqrtf(v+1e-5f);
  for(int i=threadIdx.x;i<D;i+=256) y[r*D+i]=f2b((x[r*D+i]-m)*rs);
}
__global__ void gemm_k(const bf16* A,const bf16* B,float* C,int M,int N,int K){
  __shared__ float As[16][17], Bs[16][17];
  int tx=threadIdx.x&15, ty=threadIdx.x>>4; int row=blockIdx.y*16+ty, col=blockIdx.x*16+tx;
  float acc=0;
  for(int k0=0;k0<K;k0+=16){
    As[ty][tx]=b2f(A[row*K+k0+tx]); Bs[ty][tx]=b2f(B[(k0+ty)*N+col]); __syncthreads();
    for(int k=0;k<16;k++) acc+=As[ty][k]*Bs[k][tx];
    __syncthreads();
  }
  C[row*N+col]=acc;
}
__global__ void cvt_in(const bf16* x,float* y,int n){int i=blockIdx.x*256+threadIdx.x; if(i<n) y[i]=b2f(x[i]);}
__global__ void cvt_out(const float* x,bf16* y,int n){int i=blockIdx.x*256+threadIdx.x; if(i<n) y[i]=f2b(x[i]);}
__global__ void phi_k(float* qkv){
  int i=blockIdx.x*256+threadIdx.x; if(i>=S*3*D) return; int c=i%(3*D); float t=b2f(f2b(qkv[i]));
  if(c<2*D){ t = (t>0? t+1.f: expf(t))+1e-6f; t=b2f(f2b(t)); }
  qkv[i]=t;
}
__global__ void kv_k(const float* qkv,float* kv,float* z){
  int h=blockIdx.x; int d=threadIdx.x/19, e=threadIdx.x%19; if(d>=18) return;
  float a=0; for(int s=0;s<S;s++){ float pk=qkv[s*3*D+D+h*DH+d]; float v= e<18? qkv[s*3*D+2*D+h*DH+e]:1.f; a+=pk*v; }
  if(e<18) kv[(h*18+d)*18+e]=a; else z[h*18+d]=a;
}
__global__ void o_k(const float* qkv,const float* kv,const float* z,bf16* o){
  int i=blockIdx.x*256+threadIdx.x; if(i>=S*D) return; int s=i/D, c=i%D, h=c/DH, e=c%DH;
  float num=0,den=0; for(int d=0;d<18;d++){ float q=qkv[s*3*D+h*DH+d]; num+=q*kv[(h*18+d)*18+e]; den+=q*z[h*18+d]; }
  o[i]=f2b(num/(den+1e-6f));
}
__global__ void add_k(float* x,const float* y,int n){int i=blockIdx.x*256+threadIdx.x; if(i<n) x[i]+=y[i];}
__global__ void geglu_k(const float* g,const float* u,bf16* h,int n){int i=blockIdx.x*256+threadIdx.x; if(i<n){ float x=g[i]; float t=tanhf(0.7978845608028654f*(x+0.044715f*x*x*x)); h[i]=f2b(0.5f*x*(1.f+t)*u[i]);}}

extern "C" void kernel_launch(void* const* d_in, const int* in_sizes, int n_in, void* d_out, int out_size, void* d_ws, size_t ws_size, hipStream_t stream) {
  const bf16* x=(const bf16*)d_in[0]; const bf16* wqkv=(const bf16*)d_in[1]; const bf16* wo=(const bf16*)d_in[2];
  const bf16* wg=(const bf16*)d_in[4]; const bf16* wu=(const bf16*)d_in[5]; const bf16* wd=(const bf16*)d_in[6];
  char* ws=(char*)d_ws; size_t off=0; auto al=[&](size_t b){ void* p=ws+off; off+=(b+255)/256*256; return p;};
  float* xf=(float*)al(S*D*4); bf16* ln=(bf16*)al(S*D*2); float* qkv=(float*)al((size_t)S*3*D*4); float* kv=(float*)al(H*18*18*4); float* z=(float*)al(H*18*4);
  bf16* o=(bf16*)al(S*D*2); float* y=(float*)al(S*D*4); float* g=(float*)al((size_t)S*F*4); float* u=(float*)al((size_t)S*F*4); bf16* hh=(bf16*)al((size_t)S*F*2);
  int n=S*D;
  cvt_in<<<(n+255)/256,256,0,stream>>>(x,xf,n);
  for(int l=0;l<2;l++){
    ln_k<<<S,256,0,stream>>>(xf,ln);
    gemm_k<<<dim3(3*D/16,S/16),256,0,stream>>>(ln,wqkv,qkv,S,3*D,D);
    phi_k<<<(S*3*D+255)/256,256,0,stream>>>(qkv);
    kv_k<<<H,18*19,0,stream>>>(qkv,kv,z);
    o_k<<<(n+255)/256,256,0,stream>>>(qkv,kv,z,o);
    gemm_k<<<dim3(D/16,S/16),256,0,stream>>>(o,wo,y,S,D,D);
    add_k<<<(n+255)/256,256,0,stream>>>(xf,y,n);
    ln_k<<<S,256,0,stream>>>(xf,ln);
    gemm_k<<<dim3(F/16,S/16),256,0,stream>>>(ln,wg,g,S,F,D);
    gemm_k<<<dim3(F/16,S/16),256,0,stream>>>(ln,wu,u,S,F,D);
    geglu_k<<<(S*F+255)/256,256,0,stream>>>(g,u,hh,S*F);
    gemm_k<<<dim3(D/16,S/16),256,0,stream>>>(hh,wd,y,S,D,F);
    add_k<<<(n+255)/256,256,0,stream>>>(xf,y,n);
  }
  cvt_out<<<(n+255)/256,256,0,stream>>>(xf,(bf16*)d_out,n);
}
```

```cpp
#include <hip/hip_runtime.h>
#include <hip/hip_bf16.h>
#include <stdint.h>

#define LAS __attribute__((address_space(3)))
#define GAS __attribute__((address_space(1)))
#define DI __device__ __forceinline__
typedef __attribute__((ext_vector_type(8))) __bf16 bf16x8;
typedef __attribute__((ext_vector_type(4))) __bf16 bf16x4;
typedef __attribute__((ext_vector_type(2))) __bf16 bf16x2;
typedef __attribute__((ext_vector_type(4))) short s16x4;
typedef __attribute__((ext_vector_type(8))) short s16x8;
typedef __attribute__((ext_vector_type(4))) float f32x4;
typedef __attribute__((ext_vector_type(2))) float f32x2;
typedef __attribute__((ext_vector_type(4))) unsigned u32x4;
typedef __attribute__((ext_vector_type(2))) unsigned u32x2;
typedef unsigned short u16;

constexpr int S_ = 2048, D_ = 576, F_ = 4608;
#define MFMA16(a, b, c) __builtin_amdgcn_mfma_f32_16x16x32_bf16((a), (b), (c), 0, 0, 0)

DI float bf2f(u16 b) { return __uint_as_float((unsigned)b << 16); }
DI u16 f2bf(float f) { __bf16 b = (__bf16)f; return __builtin_bit_cast(u16, b); }
DI unsigned pk2(float a, float b) { f32x2 v = {a, b}; bf16x2 r = __builtin_convertvector(v, bf16x2); return __builtin_bit_cast(unsigned, r); }
DI float lo16(unsigned u) { return __uint_as_float(u << 16); }
DI float hi16(unsigned u) { return __uint_as_float(u & 0xffff0000u); }
__host__ __device__ __forceinline__ size_t hf(int t, int c, int C) { return (((size_t)(t >> 4) * (C >> 5) + (c >> 5)) * 64 + ((c >> 2) & 3) * 16 + (t & 15)) * 8 + ((c >> 4) & 1) * 4 + (c & 3); }

DI void glds16(unsigned voff, const void* sbase, unsigned lds) {
  asm volatile("s_mov_b32 m0, %2\n\ts_nop 0\n\tglobal_load_lds_dwordx4 %0, %1" :: "v"(voff), "s"(sbase), "s"(lds) : "memory");
}
#define WAITV(n) asm volatile("s_waitcnt vmcnt(%0)" :: "n"(n) : "memory")
#define WAITL0() asm volatile("s_waitcnt lgkmcnt(0)" ::: "memory")
#define BAR() do { asm volatile("" ::: "memory"); __builtin_amdgcn_s_barrier(); asm volatile("" ::: "memory"); } while (0)

DI bf16x8 ld_frag(LAS char* p) { return __builtin_bit_cast(bf16x8, *(LAS u32x4*)p); }
#define STAMP(i) do {} while (0)
#define SB0() __builtin_amdgcn_sched_barrier(0)
#define SGB(m, n) __builtin_amdgcn_sched_group_barrier((m), (n), 0)
DI bf16x8 ld_tr(LAS char* p0, LAS char* p1) {
  s16x4 a = __builtin_amdgcn_ds_read_tr16_b64_v4i16((LAS s16x4*)p0);
  s16x4 b = __builtin_amdgcn_ds_read_tr16_b64_v4i16((LAS s16x4*)p1);
  s16x8 r = __builtin_shufflevector(a, b, 0, 1, 2, 3, 4, 5, 6, 7);
  return __builtin_bit_cast(bf16x8, r);
}
DI float geglu1(float g, float u) {
  const float c1 = -2.0f * 0.7978845608028654f * 1.4426950408889634f;
  float p = __builtin_fmaf(g * g, c1 * 0.044715f, c1);
  float e = __builtin_amdgcn_exp2f(p * g);
  float r = __builtin_amdgcn_rcpf(1.0f + e);
  return g * u * r;
}

constexpr int XSLOT = 18432;
constexpr int C_LDS = 4 * XSLOT + 5 * 16384 + 304 * 16;
#define AMF(acc, a, b) asm volatile("v_mfma_f32_16x16x32_bf16 %0, %1, %2, %0" : "+v"(acc) : "v"(a), "v"(b))
#define AMF0(acc, a, b) asm volatile("v_mfma_f32_16x16x32_bf16 %0, %1, %2, 0" : "=&v"(acc) : "v"(a), "v"(b))
#define WAITLG(n) __builtin_amdgcn_s_waitcnt(0xC07F | ((n) << 8))
#define PIN(x) asm volatile("" : "+v"(x))
DI void gstore8(unsigned voff, const void* sbase, u32x2 v) { asm volatile("global_store_dwordx2 %0, %1, %2" :: "v"(voff), "v"(v), "s"(sbase) : "memory"); }
constexpr int cstep(int k) { return k > 17 ? 0 : 2 + ((k & 1) ? 0 : 1); }
constexpr int csum(int a, int b) { int r = 0; for (int k = a; k <= b; ++k) r += cstep(k); return r; }

__global__ __launch_bounds__(512, 2) void k_c(const u16* __restrict__ x1, const float* __restrict__ st, const u16* __restrict__ Wg,
                                              const u16* __restrict__ Wu, u16* __restrict__ h, unsigned long long* dbg) {
  extern __shared__ __attribute__((aligned(16))) char smem_[];
  LAS char* smem = (LAS char*)smem_;
  const int tid = threadIdx.x, lane = tid & 63;
  const int w = __builtin_amdgcn_readfirstlane(tid >> 6);
  const int xcd = blockIdx.x & 7, idx = blockIdx.x >> 3;
  const int start = (63 * xcd + 1) >> 1, cnt = ((63 * (xcd + 1) + 1) >> 1) - start;
  if (idx >= cnt) return;
  STAMP(0);
  const int L = start + idx, slice = L / 7, range = L % 7;
  const int ntile = range < 2 ? 19 : 18, tile0 = range < 2 ? 19 * range : 38 + 18 * (range - 2);
  const unsigned ldsb = (unsigned)(size_t)smem;
  const int g = lane >> 4, t16 = lane & 15;

  u32x4 s0, s1;
  { const unsigned so = (unsigned)min(tid, ntile * 16 - 1) * 32u; const float* sb = st + (size_t)tile0 * 16 * 8;
    asm volatile("global_load_dwordx4 %0, %1, %2" : "=v"(s0) : "v"(so), "s"(sb) : "memory");
    asm volatile("global_load_dwordx4 %0, %1, %2 offset:16" : "=v"(s1) : "v"(so), "s"(sb) : "memory"); }

  const char* xb = (const char*)x1 + (size_t)tile0 * XSLOT;
  const unsigned vlin = lane * 16;
  const int row32 = 4 * w + g;
  const unsigned wvoff = row32 * (F_ * 2) + ((t16 ^ (2 * (row32 & 7))) * 16);
  const char* wgb = (const char*)Wg + slice * 256;
  const char* wub = (const char*)Wu + slice * 256;
  constexpr int WR0 = 4 * XSLOT;
  auto wslot = [&](int k) -> int { return WR0 + ((k + 2) % 5) * 16384; };
  auto issueW = [&](int k, int mat) {
    glds16(wvoff, (mat ? wub : wgb) + (size_t)k * 32 * (F_ * 2), ldsb + wslot(k) + mat * 8192 + w * 1024);
  };
  auto issueX = [&](int s) {
    const int o = (w & 3) * XSLOT + (s + (w >> 2)) * 1024; glds16(vlin, xb + o, ldsb + o);
  };
  const unsigned vp0 = vlin + w * 1024, vp1 = vp0 + 8192, vp2 = vp0 + 16384;
  const unsigned ldw = ldsb + w * 1024;
  auto load_piece = [&](int T, int pc) {
    const int Tc = min(T, ntile - 1);
    const char* src = xb + Tc * XSLOT; const unsigned dst = ldw + (T & 7) * XSLOT + pc * 8192;
    if (pc < 2 || w < 2) glds16(pc == 0 ? vp0 : pc == 1 ? vp1 : vp2, src, dst);
  };
  constexpr int LA = 5;
#pragma unroll
  for (int s = 0; s < LA; ++s) { if ((s & 1) == 0) issueX(s); issueW(s, 0); issueW(s, 1); if (s < LA - 1) BAR(); }

  bf16x8 wg[18], wu[18];
  f32x4 a1g[4], a1u[4], cg, cu;
  const u32x4 ones_u = {0x3f803f80u, 0x3f803f80u, 0x3f803f80u, 0x3f803f80u};
  bf16x8 ones = __builtin_bit_cast(bf16x8, ones_u);
  PIN(ones);
  const int q = t16 >> 2, p = lane & 3, rrow = 4 * g + q;
  const unsigned tr_off = rrow * 256 + (((2 * w + (p >> 1)) ^ (2 * (rrow & 7))) * 16) + 8 * (p & 1);
  LAS char* xl = smem + lane * 16;
  LAS char* wtr = smem + tr_off;
  bf16x8 xf1[2][4];
  constexpr int STATS = WR0 + 5 * 16384;
  {
    asm volatile("s_waitcnt vmcnt(%2)" : "+v"(s0), "+v"(s1) : "n"(csum(2, LA - 1)) : "memory");
    STAMP(1);
    float sum = __uint_as_float(s0[0]) + __uint_as_float(s0[2]) + __uint_as_float(s1[0]) + __uint_as_float(s1[2]);
    float sq = __uint_as_float(s0[1]) + __uint_as_float(s0[3]) + __uint_as_float(s1[1]) + __uint_as_float(s1[3]);
    float mean = sum * (1.0f / D_), var = sq * (1.0f / D_) - mean * mean;
    const float irs2 = fmaxf(var, 0.f) + 1e-5f, rs = __builtin_amdgcn_rsqf(irs2);
    const float c1 = -2.0f * 0.7978845608028654f * 1.4426950408889634f;
    if (tid < 304) *(LAS f32x4*)(smem + STATS + tid * 16) = f32x4{-mean, c1 * rs, c1 * 0.044715f * rs * rs * rs, irs2};
    WAITL0(); BAR();
    wg[0] = ld_tr(wtr + wslot(0), wtr + wslot(0) + 4096);
    wu[0] = ld_tr(wtr + wslot(0) + 8192, wtr + wslot(0) + 8192 + 4096);
#pragma unroll
    for (int T = 0; T < 4; ++T) xf1[0][T] = ld_frag(xl + T * XSLOT);
  }
#define P1MF(acc, W_, X_) do { if (s == 0) AMF0(acc, W_, X_); else AMF(acc, W_, X_); } while (0)
  const int half = w >> 2;
  auto vop = [&](int s, int n) {
    const int k = s + LA;
    if (k < 18) {
      if (n == 0) { if ((k & 1) == 0) issueX(k); }
      else if (n == 1) issueW(k, 0);
      else issueW(k, 1);
    } else {
      if (s >= 14) load_piece(s - 10, n);
    }
  };
#pragma unroll
  for (int s = 0; s < 18; ++s) {
    SB0();
    P1MF(a1g[0], wg[s], xf1[s & 1][0]);
    if (s < 17) wg[s + 1] = ld_tr(wtr + wslot(s + 1), wtr + wslot(s + 1) + 4096);
    if (half == 0) vop(s, 0);
    SB0();
    P1MF(a1u[0], wu[s], xf1[s & 1][0]);
    if (s < 17) wu[s + 1] = ld_tr(wtr + wslot(s + 1) + 8192, wtr + wslot(s + 1) + 8192 + 4096);
    SB0();
    P1MF(a1g[1], wg[s], xf1[s & 1][1]);
    if (s < 17) xf1[(s + 1) & 1][0] = ld_frag(xl + 0 * XSLOT + (s + 1) * 1024);
    if (half == 1) vop(s, 0);
    SB0();
    P1MF(a1u[1], wu[s], xf1[s & 1][1]);
    if (s < 17) xf1[(s + 1) & 1][1] = ld_frag(xl + 1 * XSLOT + (s + 1) * 1024);
    if (half == 0) vop(s, 1);
    SB0();
    P1MF(a1g[2], wg[s], xf1[s & 1][2]);
    if (s < 17) xf1[(s + 1) & 1][2] = ld_frag(xl + 2 * XSLOT + (s + 1) * 1024);
    SB0();
    P1MF(a1u[2], wu[s], xf1[s & 1][2]);
    if (s < 17) xf1[(s + 1) & 1][3] = ld_frag(xl + 3 * XSLOT + (s + 1) * 1024);
    if (half == 1) vop(s, 1);
    SB0();
    P1MF(a1g[3], wg[s], xf1[s & 1][3]);
    if (half == 0) vop(s, 2);
    SB0();
    P1MF(a1u[3], wu[s], xf1[s & 1][3]);
    SB0();
    P1MF(cg, wg[s], ones);
    if (half == 1) vop(s, 2);
    SB0();
    P1MF(cu, wu[s], ones);
    SB0();
    if (s < 17) {
      if (s + 2 < 18) {
        switch (s) {
#define WCASE(S) case S: WAITV(csum(S + 3, (S + LA < 17 ? S + LA : 17)) + (S >= 14 ? (S - 13) * 2 : 0)); break;
          WCASE(0) WCASE(1) WCASE(2) WCASE(3) WCASE(4) WCASE(5) WCASE(6) WCASE(7) WCASE(8) WCASE(9) WCASE(10) WCASE(11) WCASE(12) WCASE(13) WCASE(14) WCASE(15)
#undef WCASE
        }
      }
      WAITL0(); BAR();
    }
  }
  STAMP(2);
  WAITV(6); WAITL0(); BAR();

  const char* hb = (const char*)h + ((size_t)tile0 * (F_ / 32) + slice * 4 + (w >> 1)) * 1024;
  const unsigned hvo = lane * 16 + (w & 1) * 8;
  constexpr size_t HT = (size_t)(F_ / 32) * 1024;
  auto epi = [&](const f32x4& ag, const f32x4& au, int T) -> u32x2 {
    f32x4 sv = *(LAS f32x4*)(smem + STATS + (T * 16 + t16) * 16);
    float hv[4];
#pragma unroll
    for (int i = 0; i < 4; ++i) {
      float tg = __builtin_fmaf(sv[0], cg[i], ag[i]), tu = __builtin_fmaf(sv[0], cu[i], au[i]);
      float y = __builtin_fmaf(tg * tg, sv[2], sv[1]) * tg;
      float e = __builtin_amdgcn_exp2f(y);
      float r = __builtin_amdgcn_rcpf(__builtin_fmaf(e, sv[3], sv[3]));
      hv[i] = tg * tu * r;
    }
    return u32x2{pk2(hv[0], hv[1]), pk2(hv[2], hv[3])};
  };
  constexpr int PD = 4;
  bf16x8 xr[6];
#pragma unroll
  for (int s = 0; s < PD; ++s) xr[s] = ld_frag(xl + 4 * XSLOT + s * 1024);
  asm volatile("s_nop 7\n\ts_nop 1" : "+v"(a1g[0]), "+v"(a1g[1]), "+v"(a1g[2]), "+v"(a1g[3]), "+v"(a1u[0]), "+v"(a1u[1]), "+v"(a1u[2]), "+v"(a1u[3]), "+v"(cg), "+v"(cu));
#pragma unroll
  for (int T = 0; T < 3; ++T) { u32x2 o = epi(a1g[T], a1u[T], T); gstore8(hvo, hb + (size_t)T * HT, o); }
  STAMP(3);
  const int ph = half;
  auto tile = [&](int T, f32x4& ag, f32x4& au, const f32x4& pg, const f32x4& pu) {
    LAS char* xt = xl + (T & 7) * XSLOT;
    LAS char* xn = xl + ((T + 1) & 7) * XSLOT;
    f32x4 sv = *(LAS f32x4*)(smem + STATS + ((T - 1) * 16 + t16) * 16);
    float tg, tu, y, e, m, r, hv[4];
    unsigned o0 = 0, o1 = 0;
    const char* nsrc = xb + min(T + 4, ntile - 1) * XSLOT; const unsigned ndst = ldw + ((T + 4) & 7) * XSLOT;
    SB0();
#pragma unroll
    for (int s = 0; s < 18; ++s) {
      if (s == 8) {
        WAITV(4);
        BAR();
        SB0();
      }
      if (s + PD < 18) xr[(s + PD) % 6] = ld_frag(xt + (s + PD) * 1024); else xr[(s + PD) % 6] = ld_frag(xn + (s + PD - 18) * 1024);
      if (s == 0) AMF0(ag, wg[0], xr[0]); else AMF(ag, wg[s], xr[s % 6]);
      if (s >= 2 && s <= 17) {
        const int k = s - 2, i = k >> 2, stg = k & 3;
        if (stg == 0) { if (i > 0) { hv[i - 1] = m * r; PIN(hv[i - 1]); } tg = __builtin_fmaf(sv[0], cg[i], pg[i]); tu = __builtin_fmaf(sv[0], cu[i], pu[i]); PIN(tg); PIN(tu); }
        else if (stg == 1) { float t2 = tg * tg; float pp = __builtin_fmaf(t2, sv[2], sv[1]); y = pp * tg; PIN(y); }
        else if (stg == 2) { e = __builtin_amdgcn_exp2f(y); m = tg * tu; PIN(e); PIN(m); }
        else { float d = __builtin_fmaf(e, sv[3], sv[3]); r = __builtin_amdgcn_rcpf(d); PIN(r); }
      }
      if (s == 0) AMF0(au, wu[0], xr[0]); else AMF(au, wu[s], xr[s % 6]);
      if (s >= 9 && s <= 14) { const int pc = (s - 9) >> 1; if (((s - 9) & 1) == ph && (pc < 2 || w < 2)) glds16(pc == 0 ? vp0 : pc == 1 ? vp1 : vp2, nsrc, ndst + pc * 8192); }
      SB0();
    }
    hv[3] = m * r; o0 = pk2(hv[0], hv[1]); o1 = pk2(hv[2], hv[3]);
    gstore8(hvo, hb + (size_t)(T - 1) * HT, u32x2{o0, o1});
  };
  f32x4 Ag, Au, Bg, Bu;
  int T = 4;
  if (ntile == 19) { tile(4, Bg, Bu, a1g[3], a1u[3]); T = 5; } else { Bg = a1g[3]; Bu = a1u[3]; }
  for (; T < ntile; T += 2) {
    tile(T, Ag, Au, Bg, Bu);
    tile(T + 1, Bg, Bu, Ag, Au);
  }
  STAMP(4);
  asm volatile("s_nop 7\n\ts_nop 1" : "+v"(Bg), "+v"(Bu));
  { u32x2 o = epi(Bg, Bu, ntile - 1); gstore8(hvo, hb + (size_t)(ntile - 1) * HT, o); }
  STAMP(5);
}

constexpr int D_STAGE = 2 * 2 * 12288;
constexpr int D_LDS = 3 * D_STAGE;
__global__ __launch_bounds__(512, 2) void k_d(const u16* __restrict__ h, const u16* __restrict__ Wd, const u16* __restrict__ x1, u16* __restrict__ part, unsigned long long* dbg) {
  extern __shared__ __attribute__((aligned(16))) char smem_[];
  LAS char* smem = (LAS char*)smem_;
  const int tid = threadIdx.x, lane = tid & 63;
  const int w = __builtin_amdgcn_readfirstlane(tid >> 6);
  const int xcd = blockIdx.x & 7, idx = blockIdx.x >> 3;
  const int ks = xcd & 3, tt = (xcd >> 2) * 8 + (idx >> 2), nt = idx & 3;
  const int mq = w & 3, kh = w >> 2;
  const unsigned ldsb = (unsigned)(size_t)smem;
  const int g = lane >> 4, t16 = lane & 15;
  STAMP(0);
  unsigned wv[3];
#pragma unroll
  for (int i = 0; i < 3; ++i) { int qq = 64 * i + lane, k = qq / 24, pp = qq % 24; int c = (pp & ~7) | ((pp & 7) ^ (2 * ((k >> 1) & 3))); wv[i] = k * (D_ * 2) + c * 16; }
  const int krow0 = ks * 1152 + kh * 576;
  const char* wb = (const char*)Wd + (size_t)(krow0 + 8 * mq) * (D_ * 2) + nt * 256;
  const int inner = nt * 32;
  const int rot = 0;
  auto iof = [&](int it) { int k = it + rot; return k >= 9 ? k - 9 : k; };
  auto stageW1 = [&](int it, int n) {
    const int u = n / 3, i = n % 3;
    const unsigned dst = ldsb + (it % 3) * D_STAGE + kh * 24576 + mq * 3072;
    glds16(wv[i], wb + (size_t)(iof(it) * 2 + u) * 32 * (D_ * 2), dst + u * 12288 + i * 1024);
  };
  const char* hb = (const char*)h + ((size_t)(tt * 8 + 2 * mq) * (F_ / 32) + krow0 / 32) * 1024;
  const unsigned hv = lane * 16;
  u32x4 hr[3][4];
  auto loadH1 = [&](int it, int n, u32x4& r) {
    const int u = n >> 1, m = n & 1;
    asm volatile("global_load_dwordx4 %0, %1, %2" : "=v"(r) : "v"(hv), "s"(hb + (size_t)m * (F_ / 32) * 1024 + (iof(it) * 2 + u) * 1024) : "memory");
  };
#pragma unroll
  for (int n = 0; n < 4; ++n) loadH1(0, n, hr[0][n]);
#pragma unroll
  for (int n = 0; n < 6; ++n) stageW1(0, n);
#pragma unroll
  for (int n = 0; n < 6; ++n) stageW1(1, n);
#pragma unroll
  for (int n = 0; n < 4; ++n) loadH1(1, n, hr[1][n]);
  stageW1(2, 0);
  f32x4 acc[2][9];
  const int q = t16 >> 2, p = lane & 3, rr = 4 * g + q;
  const int sw = 2 * ((rr >> 1) & 3);
  LAS char* wl = smem + kh * 24576 + rr * 384 + 8 * (p & 1);
  unsigned poff[9];
#pragma unroll
  for (int j = 0; j < 9; ++j) { const int c = inner / 16 + 2 * j + (p >> 1); poff[j] = ((c & ~7) | ((c & 7) ^ sw)) * 16; }
  constexpr int WPD = 3;
  bf16x8 wf[6];
  auto rdW = [&](int it, int i) -> bf16x8 { const int u = i / 9, j = i % 9; LAS char* a = wl + (it % 3) * D_STAGE + u * 12288 + poff[j]; return ld_tr(a, a + 16 * 384); };
  asm volatile("s_waitcnt vmcnt(11)" : "+v"(hr[0][0]), "+v"(hr[0][1]), "+v"(hr[0][2]), "+v"(hr[0][3]) :: "memory");
  STAMP(1);
  BAR();
#pragma unroll
  for (int i = 0; i < WPD; ++i) wf[i] = rdW(0, i);
#pragma unroll
  for (int it = 0; it < 9; ++it) {
    u32x4(&r)[4] = hr[it % 3];
    SB0();
#pragma unroll
    for (int i = 0; i < 18; ++i) {
      const int u = i / 9, j = i % 9;
      if (i == 18 - WPD && it < 8) {
        u32x4(&rn)[4] = hr[(it + 1) % 3];
        if (it < 7) asm volatile("s_waitcnt vmcnt(9) lgkmcnt(0)" : "+v"(rn[0]), "+v"(rn[1]), "+v"(rn[2]), "+v"(rn[3]) :: "memory");
        else asm volatile("s_waitcnt vmcnt(0) lgkmcnt(0)" : "+v"(rn[0]), "+v"(rn[1]), "+v"(rn[2]), "+v"(rn[3]) :: "memory");
        BAR();
        SB0();
      }
      if (i + WPD < 18) wf[(i + WPD) % 6] = rdW(it, i + WPD);
      else if (it < 8) wf[(i + WPD) % 6] = rdW(it + 1, i + WPD - 18);
      if (it == 0 && u == 0) { AMF0(acc[0][j], wf[i % 6], r[u * 2 + 0]); AMF0(acc[1][j], wf[i % 6], r[u * 2 + 1]); }
      else { AMF(acc[0][j], wf[i % 6], r[u * 2 + 0]); AMF(acc[1][j], wf[i % 6], r[u * 2 + 1]); }
      if (it + 2 < 9) {
        if (i <= 8 && (i & 1) == 0) { if (i > 0 || true) { const int n = 1 + i / 2; if (n <= 5) stageW1(it + 2, n); } }
        if (i == 10 || i == 12 || i == 14) loadH1(it + 2, (i - 10) / 2, hr[(it + 2) % 3][(i - 10) / 2]);
        if (i == 16) loadH1(it + 2, 3, hr[(it + 2) % 3][3]);
      }
      if (i == 17 && it + 3 < 9) stageW1(it + 3, 0);
      SB0();
    }
  }
  STAMP(2);
  asm volatile("s_nop 7\n\ts_nop 1" ::: "memory");
  WAITL0(); BAR();
  LAS f32x4* red = (LAS f32x4*)smem;
#pragma unroll
  for (int m = 0; m < 2; ++m)
#pragma unroll
    for (int j = 0; j < 9; ++j) { const bool mine = (kh == 0) ? (j < 5) : (j >= 5); if (!mine) { PIN(acc[m][j]); red[((mq * 2 + m) * 9 + j) * 64 + lane] = acc[m][j]; } }
  u32x2 xres[2][5];
  if (ks == 0) {
#pragma unroll
    for (int m = 0; m < 2; ++m)
#pragma unroll
      for (int jj = 0; jj < 5; ++jj) {
        const int j = kh == 0 ? jj : min(5 + jj, 8);
        const int cb = nt * 9 + j;
        const size_t blk = (((size_t)(tt * 8 + 2 * mq + m) * (D_ / 32) + (cb >> 1)) * 64 + lane) * 8 + (cb & 1) * 4;
        xres[m][jj] = *(const GAS u32x2*)(x1 + blk);
      }
  } else {
#pragma unroll
    for (int m = 0; m < 2; ++m)
#pragma unroll
      for (int jj = 0; jj < 5; ++jj) xres[m][jj] = u32x2{0, 0};
  }
  WAITL0(); BAR();
#pragma unroll
  for (int m = 0; m < 2; ++m)
#pragma unroll
    for (int j = 0; j < 9; ++j) {
      const bool mine = (kh == 0) ? (j < 5) : (j >= 5);
      if (mine) {
        const int jj = j < 5 ? j : j - 5;
        f32x4 v = acc[m][j] + red[((mq * 2 + m) * 9 + j) * 64 + lane];
        const int cb = nt * 9 + j;
        const size_t blk = (((size_t)(tt * 8 + 2 * mq + m) * (D_ / 32) + (cb >> 1)) * 64 + lane) * 8 + (cb & 1) * 4;
        u32x2 xr = xres[m][jj];
        v[0] += lo16(xr[0]); v[1] += hi16(xr[0]); v[2] += lo16(xr[1]); v[3] += hi16(xr[1]);
        *(GAS u32x2*)(part + (size_t)ks * S_ * D_ + blk) = u32x2{pk2(v[0], v[1]), pk2(v[2], v[3])};
      }
    }
  STAMP(3);
}

template <int MODE>
__global__ __launch_bounds__(576) void k_r(const u16* __restrict__ src, u16* __restrict__ xr, u16* __restrict__ xn, u16* __restrict__ outp,
                                           const u16* __restrict__ wqkv, u16* __restrict__ wt) {
  const int tid = threadIdx.x, lane = tid & 63, w = tid >> 6;
  if (MODE == 0 && blockIdx.x >= 256) {
    const int f = (blockIdx.x - 256) * 9 + w;
    if (f < 16 * 7 * 18) {
      const int s = f % 18, j = (f / 18) % 7, hp = f / 126;
      const int n16 = lane & 15, g = lane >> 4;
      int col; bool valid = true;
      if (j < 6) col = (j >> 1) * 576 + 36 * hp + (j & 1) * 16 + n16;
      else { const int sec = n16 >> 2; valid = sec < 3; col = (valid ? sec : 0) * 576 + 36 * hp + 32 + (n16 & 3); }
      unsigned v[8];
#pragma unroll
      for (int e = 0; e < 8; ++e) { const int k = 32 * s + 16 * (e >> 2) + 4 * g + (e & 3); v[e] = valid ? wqkv[(size_t)k * 1728 + col] : 0; }
      u32x4 o = {v[0] | (v[1] << 16), v[2] | (v[3] << 16), v[4] | (v[5] << 16), v[6] | (v[7] << 16)};
      *(u32x4*)(wt + ((size_t)f * 64 + lane) * 8) = o;
    }
    return;
  }
  __shared__ float red[9][8][2];
  const int t8 = tid & 7, cg = tid >> 3, c32 = cg >> 2, g = cg & 3;
  const int t = blockIdx.x * 8 + t8;
  const size_t ffo = (((size_t)(t >> 4) * 18 + c32) * 64 + g * 16 + (t & 15)) * 8;
  float v[8];
  if (MODE == 0) {
    const u32x2 a = *(const u32x2*)(src + (size_t)t * D_ + 32 * c32 + 4 * g), b = *(const u32x2*)(src + (size_t)t * D_ + 32 * c32 + 16 + 4 * g);
    v[0] = lo16(a[0]); v[1] = hi16(a[0]); v[2] = lo16(a[1]); v[3] = hi16(a[1]); v[4] = lo16(b[0]); v[5] = hi16(b[0]); v[6] = lo16(b[1]); v[7] = hi16(b[1]);
  } else {
    u32x4 p[4];
#pragma unroll
    for (int i = 0; i < 4; ++i) p[i] = *(const u32x4*)(src + (size_t)i * S_ * D_ + ffo);
#pragma unroll
    for (int e = 0; e < 4; ++e) {
      v[2 * e] = (lo16(p[0][e]) + lo16(p[1][e])) + (lo16(p[2][e]) + lo16(p[3][e]));
      v[2 * e + 1] = (hi16(p[0][e]) + hi16(p[1][e])) + (hi16(p[2][e]) + hi16(p[3][e]));
    }
  }
  u32x4 xb = {pk2(v[0], v[1]), pk2(v[2], v[3]), pk2(v[4], v[5]), pk2(v[6], v[7])};
  if (MODE == 2) {
    *(u32x2*)(outp + (size_t)t * D_ + 32 * c32 + 4 * g) = u32x2{xb[0], xb[1]};
    *(u32x2*)(outp + (size_t)t * D_ + 32 * c32 + 16 + 4 * g) = u32x2{xb[2], xb[3]};
    return;
  }
  *(u32x4*)(xr + ffo) = xb;
#pragma unroll
  for (int e = 0; e < 4; ++e) { v[2 * e] = lo16(xb[e]); v[2 * e + 1] = hi16(xb[e]); }
  float m = ((v[0] + v[1]) + (v[2] + v[3]) + (v[4] + v[5]) + (v[6] + v[7])) * 0.125f, M2 = 0.f;
#pragma unroll
  for (int e = 0; e < 8; ++e) { const float d = v[e] - m; M2 = __builtin_fmaf(d, d, M2); }
  float n = 8.f;
#pragma unroll
  for (int o = 8; o < 64; o <<= 1) {
    const float m2 = __shfl_xor(m, o), q2 = __shfl_xor(M2, o), d = m - m2;
    M2 = M2 + q2 + d * d * (n * 0.5f); m = 0.5f * (m + m2); n *= 2.f;
  }
  if (lane < 8) { red[w][lane][0] = m; red[w][lane][1] = M2; }
  __syncthreads();
  float mt = 0.f;
#pragma unroll
  for (int i = 0; i < 9; ++i) mt += red[i][t8][0];
  mt *= (1.0f / 9.0f);
  float Mt = 0.f;
#pragma unroll
  for (int i = 0; i < 9; ++i) { const float d = red[i][t8][0] - mt; Mt += red[i][t8][1] + d * d * 64.f; }
  const float rs = 1.0f / sqrtf(Mt * (1.0f / D_) + 1e-5f);
  u32x4 o = {pk2((v[0] - mt) * rs, (v[1] - mt) * rs), pk2((v[2] - mt) * rs, (v[3] - mt) * rs), pk2((v[4] - mt) * rs, (v[5] - mt) * rs), pk2((v[6] - mt) * rs, (v[7] - mt) * rs)};
  *(u32x4*)(xn + ffo) = o;
}

constexpr int A_STAGE = 32768;
constexpr int A_LDS = 4 * A_STAGE;
DI float phi(float t) { return (t > 0.f ? t + 1.0f : __builtin_amdgcn_exp2f(t * 1.4426950408889634f)) + 1e-6f; }
__global__ __launch_bounds__(512, 2) void k_a(const u16* __restrict__ xn, const u16* __restrict__ wt, u16* __restrict__ pq, float* __restrict__ kvp) {
  extern __shared__ __attribute__((aligned(16))) char smem_[];
  LAS char* smem = (LAS char*)smem_;
  const int tid = threadIdx.x, lane = tid & 63;
  const int w = __builtin_amdgcn_readfirstlane(tid >> 6);
  const int xcd = blockIdx.x & 7, idx = blockIdx.x >> 3;
  const int tt = (xcd >> 1) * 4 + (idx >> 3), hp = (xcd & 1) * 8 + (idx & 7);
  const unsigned ldsb = (unsigned)(size_t)smem;
  const int g = lane >> 4, t16 = lane & 15;
  const unsigned vlin = lane * 16;
  const char* xs = (const char*)xn + (size_t)(tt * 8 + w) * 18432;
  const char* wsrc = (const char*)wt + (size_t)(hp * 7 + min(w, 6)) * 18432;
  auto stage = [&](int st) {
    const unsigned dst = ldsb + (st & 3) * A_STAGE + w * 1024;
#pragma unroll
    for (int u = 0; u < 2; ++u) { glds16(vlin, xs + (2 * st + u) * 1024, dst + u * 16384); glds16(vlin, wsrc + (2 * st + u) * 1024, dst + u * 16384 + 8192); }
  };
  stage(0); BAR(); stage(1); BAR(); stage(2);
  const int tp = w & 3, cgp = w >> 2;
  f32x4 acc[2][4];
#pragma unroll
  for (int m = 0; m < 2; ++m)
#pragma unroll
    for (int j = 0; j < 4; ++j) acc[m][j] = f32x4{0, 0, 0, 0};
  LAS char* xl = smem + lane * 16 + tp * 2048;
  LAS char* wl = smem + lane * 16 + 8192 + cgp * 4096;
#pragma unroll
  for (int st = 0; st < 9; ++st) {
    if (st < 7) WAITV(8); else if (st == 7) WAITV(4); else WAITV(0);
    WAITL0(); BAR();
    if (st + 3 < 9) stage(st + 3);
#pragma unroll
    for (int u = 0; u < 2; ++u) {
      const int o = (st & 3) * A_STAGE + u * 16384;
      bf16x8 x0 = ld_frag(xl + o), x1 = ld_frag(xl + o + 1024);
#pragma unroll
      for (int j = 0; j < 4; ++j) {
        if (j < 3 || cgp == 0) {
          bf16x8 wf = ld_frag(wl + o + j * 1024);
          acc[0][j] = MFMA16(wf, x0, acc[0][j]);
          acc[1][j] = MFMA16(wf, x1, acc[1][j]);
        }
      }
    }
  }
  WAITL0(); BAR();
  constexpr int KVS = 224;
  if (tid < 128) {
    LAS char* r = smem + tid * KVS;
    *(LAS u32x2*)(r + 72) = u32x2{0, 0}; *(LAS u32x2*)(r + 80) = u32x2{0, 0}; *(LAS u32x2*)(r + 88) = u32x2{0, 0};
    *(LAS u32x2*)(r + 168) = u32x2{0x3f80u, 0}; *(LAS u32x2*)(r + 176) = u32x2{0, 0}; *(LAS u32x2*)(r + 184) = u32x2{0, 0};
  }
#pragma unroll
  for (int m = 0; m < 2; ++m) {
    const int tl = (2 * tp + m) * 16 + t16;
    const int tg = tt * 128 + tl;
    auto store_q = [&](int c, const f32x4& a) {
      const int cg4 = c >> 2;
      const size_t o = (((size_t)(tg >> 4) * 18 + (cg4 >> 3)) * 64 + (cg4 & 3) * 16 + (tg & 15)) * 8 + ((cg4 >> 2) & 1) * 4;
      *(GAS u32x2*)(pq + o) = u32x2{pk2(phi(a[0]), phi(a[1])), pk2(phi(a[2]), phi(a[3]))};
    };
    LAS char* row = smem + tl * KVS;
    if (cgp == 0) {
      store_q(36 * hp + 4 * g, acc[m][0]);
      store_q(36 * hp + 16 + 4 * g, acc[m][1]);
      *(LAS u32x2*)(row + 8 * g) = u32x2{pk2(phi(acc[m][2][0]), phi(acc[m][2][1])), pk2(phi(acc[m][2][2]), phi(acc[m][2][3]))};
      *(LAS u32x2*)(row + 32 + 8 * g) = u32x2{pk2(phi(acc[m][3][0]), phi(acc[m][3][1])), pk2(phi(acc[m][3][2]), phi(acc[m][3][3]))};
    } else {
      *(LAS u32x2*)(row + 96 + 8 * g) = u32x2{pk2(acc[m][0][0], acc[m][0][1]), pk2(acc[m][0][2], acc[m][0][3])};
      *(LAS u32x2*)(row + 96 + 32 + 8 * g) = u32x2{pk2(acc[m][1][0], acc[m][1][1]), pk2(acc[m][1][2], acc[m][1][3])};
      const f32x4 a = acc[m][2];
      if (g == 0) store_q(36 * hp + 32, a);
      else if (g == 1) *(LAS u32x2*)(row + 64) = u32x2{pk2(phi(a[0]), phi(a[1])), pk2(phi(a[2]), phi(a[3]))};
      else if (g == 2) *(LAS u32x2*)(row + 96 + 64) = u32x2{pk2(a[0], a[1]), pk2(a[2], a[3])};
    }
  }
  WAITL0(); BAR();
  const int kt = (0x02211100 >> (4 * w)) & 3, vt = (0x22121010 >> (4 * w)) & 3;
  f32x4 kv = {0, 0, 0, 0};
  const int q = t16 >> 2, p = lane & 3;
#pragma unroll
  for (int s = 0; s < 4; ++s) {
    LAS char* r0 = smem + (32 * s + 4 * g + q) * KVS + 8 * p;
    bf16x8 a = ld_tr(r0 + kt * 32, r0 + 16 * KVS + kt * 32);
    bf16x8 b = ld_tr(r0 + 96 + vt * 32, r0 + 16 * KVS + 96 + vt * 32);
    kv = MFMA16(a, b, kv);
  }
  {
    const int e = 16 * vt + t16;
#pragma unroll
    for (int i = 0; i < 4; ++i) {
      const int d = 16 * kt + 4 * g + i;
      if (d < 36) {
        const int hd = d >= 18 ? 1 : 0, dd = d - 18 * hd;
        float* base = kvp + ((size_t)tt * 32 + 2 * hp + hd) * 342;
        if (e < 36) { const int he = e >= 18 ? 1 : 0; if (he == hd) base[dd * 18 + (e - 18 * he)] = kv[i]; }
        else if (e == 36) base[324 + dd] = kv[i];
      }
    }
  }
}

__global__ __launch_bounds__(256) void k_a2(const float* __restrict__ kvp, const u16* __restrict__ wo, u16* __restrict__ mt, u16* __restrict__ zt) {
  __shared__ float kvs[3][342];
  __shared__ __attribute__((aligned(16))) u16 wos[54][48];
  const int tid = threadIdx.x, lane = tid & 63, w = tid >> 6;
  const int s = blockIdx.x % 18, cgp = blockIdx.x / 18;
  const int h0 = (32 * s) / 18, h1 = (32 * s + 31) / 18, nh = h1 - h0 + 1;
  for (int i = tid; i < nh * 342; i += 256) {
    const int hh = i / 342, r = i % 342;
    float a = 0.f;
#pragma unroll
    for (int p = 0; p < 16; ++p) a += kvp[((size_t)p * 32 + h0 + hh) * 342 + r];
    kvs[hh][r] = bf2f(f2bf(a));
  }
  for (int i = tid; i < nh * 18 * 6; i += 256) {
    const int r = i / 6, c = i % 6;
    *(u32x4*)&wos[r][c * 8] = *(const u32x4*)(wo + (size_t)(18 * h0 + r) * D_ + cgp * 48 + c * 8);
  }
  __syncthreads();
  const int n16 = lane & 15, g = lane >> 4;
  if (w < 3) {
    float o[8];
#pragma unroll
    for (int e8 = 0; e8 < 8; ++e8) {
      const int k = 32 * s + 16 * (e8 >> 2) + 4 * g + (e8 & 3);
      const int hh = k / 18 - h0, d = k % 18;
      float a = 0.f;
#pragma unroll
      for (int e = 0; e < 18; ++e) a = __builtin_fmaf(kvs[hh][d * 18 + e], bf2f(wos[hh * 18 + e][w * 16 + n16]), a);
      o[e8] = a;
    }
    const int jglob = cgp * 3 + w;
    *(u32x4*)(mt + (((size_t)jglob * 18 + s) * 64 + lane) * 8) = u32x4{pk2(o[0], o[1]), pk2(o[2], o[3]), pk2(o[4], o[5]), pk2(o[6], o[7])};
  } else if (cgp == 0) {
    const int hrow = (s < 9 ? 0 : 16) + n16;
    float o[8];
#pragma unroll
    for (int e8 = 0; e8 < 8; ++e8) {
      const int k = 32 * s + 16 * (e8 >> 2) + 4 * g + (e8 & 3);
      const int hk = k / 18;
      o[e8] = (hk == hrow) ? kvs[hk - h0][324 + k % 18] : 0.f;
    }
    *(u32x4*)(zt + ((size_t)s * 64 + lane) * 8) = u32x4{pk2(o[0], o[1]), pk2(o[2], o[3]), pk2(o[4], o[5]), pk2(o[6], o[7])};
  }
}

__global__ __launch_bounds__(576) void k_b(const u16* __restrict__ pq, const u16* __restrict__ mt, const u16* __restrict__ zt, const u16* __restrict__ xr,
                                           u16* __restrict__ x1, float* __restrict__ st) {
  __shared__ __attribute__((aligned(16))) char sq[2 * 18432];
  __shared__ float rden[32][33];
  __shared__ float sred[9][32][2];
  const int tid = threadIdx.x, lane = tid & 63;
  const int w = __builtin_amdgcn_readfirstlane(tid >> 6);
  const int xcd = blockIdx.x & 7, idx = blockIdx.x >> 3;
  const int tt = xcd * 8 + (idx >> 2), nt = idx & 3;
  const int g = lane >> 4, t16 = lane & 15;
  u32x4 mf[18];
  const u16* mb = mt + ((size_t)(nt * 9 + w) * 18 * 64 + lane) * 8;
#pragma unroll
  for (int s = 0; s < 18; ++s) mf[s] = *(const u32x4*)(mb + (size_t)s * 512);
  u32x4 pf[4];
#pragma unroll
  for (int i = 0; i < 4; ++i) pf[i] = *(const u32x4*)(pq + ((size_t)tt * 36 + w * 4 + i) * 512 + lane * 8);
  u32x4 zf[9];
  if (w < 4) {
#pragma unroll
    for (int s = 0; s < 9; ++s) zf[s] = *(const u32x4*)(zt + ((size_t)((w & 1) * 9 + s) * 64 + lane) * 8);
  }
  u32x2 xres[2];
  const int cb = nt * 9 + w;
#pragma unroll
  for (int m = 0; m < 2; ++m) xres[m] = *(const u32x2*)(xr + (((size_t)(tt * 2 + m) * 18 + (cb >> 1)) * 64 + lane) * 8 + (cb & 1) * 4);
#pragma unroll
  for (int i = 0; i < 4; ++i) *(u32x4*)(sq + (w * 4 + i) * 1024 + lane * 16) = pf[i];
  __syncthreads();
  if (w < 4) {
    f32x4 d = {0, 0, 0, 0};
#pragma unroll
    for (int s = 0; s < 9; ++s) {
      const u32x4 b = *(const u32x4*)(sq + ((w >> 1) * 18 + (w & 1) * 9 + s) * 1024 + lane * 16);
      d = MFMA16(__builtin_bit_cast(bf16x8, zf[s]), __builtin_bit_cast(bf16x8, b), d);
    }
#pragma unroll
    for (int i = 0; i < 4; ++i) rden[(w >> 1) * 16 + t16][(w & 1) * 16 + 4 * g + i] = 1.0f / (d[i] + 1e-6f);
  }
  __syncthreads();
#pragma unroll
  for (int i = 0; i < 4; ++i) {
    const int f = w * 4 + i, T = f / 18, s = f % 18;
    const float* rd = rden[T * 16 + t16];
    const int c0 = 32 * s + 4 * g;
    const float r0 = rd[(c0 * 3641) >> 16], r1 = rd[((c0 + 2) * 3641) >> 16], r2 = rd[((c0 + 16) * 3641) >> 16], r3 = rd[((c0 + 18) * 3641) >> 16];
    const u32x4 v = pf[i];
    *(u32x4*)(sq + f * 1024 + lane * 16) = u32x4{pk2(lo16(v[0]) * r0, hi16(v[0]) * r0), pk2(lo16(v[1]) * r1, hi16(v[1]) * r1), pk2(lo16(v[2]) * r2, hi16(v[2]) * r2), pk2(lo16(v[3]) * r3, hi16(v[3]) * r3)};
  }
  __syncthreads();
  f32x4 acc[2] = {{0, 0, 0, 0}, {0, 0, 0, 0}};
#pragma unroll
  for (int s = 0; s < 18; ++s) {
    const u32x4 b0 = *(const u32x4*)(sq + s * 1024 + lane * 16), b1 = *(const u32x4*)(sq + (18 + s) * 1024 + lane * 16);
    acc[0] = MFMA16(__builtin_bit_cast(bf16x8, mf[s]), __builtin_bit_cast(bf16x8, b0), acc[0]);
    acc[1] = MFMA16(__builtin_bit_cast(bf16x8, mf[s]), __builtin_bit_cast(bf16x8, b1), acc[1]);
  }
#pragma unroll
  for (int m = 0; m < 2; ++m) {
    const unsigned o0 = pk2(acc[m][0] + lo16(xres[m][0]), acc[m][1] + hi16(xres[m][0])), o1 = pk2(acc[m][2] + lo16(xres[m][1]), acc[m][3] + hi16(xres[m][1]));
    *(u32x2*)(x1 + (((size_t)(tt * 2 + m) * 18 + (cb >> 1)) * 64 + lane) * 8 + (cb & 1) * 4) = u32x2{o0, o1};
    const float a0 = lo16(o0), a1 = hi16(o0), a2 = lo16(o1), a3 = hi16(o1);
    float sm = (a0 + a1) + (a2 + a3), sq2 = (a0 * a0 + a1 * a1) + (a2 * a2 + a3 * a3);
    sm += __shfl_xor(sm, 16); sq2 += __shfl_xor(sq2, 16);
    sm += __shfl_xor(sm, 32); sq2 += __shfl_xor(sq2, 32);
    if (g == 0) { sred[w][m * 16 + t16][0] = sm; sred[w][m * 16 + t16][1] = sq2; }
  }
  __syncthreads();
  if (tid < 64) {
    const int t = tid >> 1, c = tid & 1;
    float a = 0.f;
#pragma unroll
    for (int i = 0; i < 9; ++i) a += sred[i][t][c];
    st[((size_t)(tt * 32 + t) * 4 + nt) * 2 + c] = a;
  }
}

#define FLUSHL2()
extern "C" void kernel_launch(void* const* d_in, const int* in_sizes, int n_in, void* d_out, int out_size, void* d_ws, size_t ws_size, hipStream_t stream) {
  const u16* x = (const u16*)d_in[0]; const u16* wqkv = (const u16*)d_in[1]; const u16* wo = (const u16*)d_in[2];
  const u16* wg = (const u16*)d_in[4]; const u16* wu = (const u16*)d_in[5]; const u16* wd = (const u16*)d_in[6];
  char* ws = (char*)d_ws; size_t off = 0; auto al = [&](size_t b) { void* p = ws + off; off += (b + 1023) / 1024 * 1024; return p; };
  u16* xr = (u16*)al((size_t)S_ * D_ * 2); u16* xn = (u16*)al((size_t)S_ * D_ * 2); u16* wt = (u16*)al((size_t)2016 * 1024);
  u16* pq = (u16*)al((size_t)S_ * D_ * 2); float* kvp = (float*)al((size_t)16 * 32 * 342 * 4); u16* mt = (u16*)al((size_t)36 * 18 * 1024); u16* zt = (u16*)al(18 * 1024);
  u16* x1 = (u16*)al((size_t)S_ * D_ * 2); float* st = (float*)al((size_t)S_ * 8 * 4); u16* h = (u16*)al((size_t)S_ * F_ * 2); u16* part = (u16*)al((size_t)4 * S_ * D_ * 2);
  unsigned long long* dbg = nullptr;
  (void)hipFuncSetAttribute((const void*)k_a, hipFuncAttributeMaxDynamicSharedMemorySize, A_LDS);
  (void)hipFuncSetAttribute((const void*)k_c, hipFuncAttributeMaxDynamicSharedMemorySize, C_LDS);
  (void)hipFuncSetAttribute((const void*)k_d, hipFuncAttributeMaxDynamicSharedMemorySize, D_LDS);
  for (int l = 0; l < 2; ++l) {
    if (l == 0) k_r<0><<<256 + 224, 576, 0, stream>>>(x, xr, xn, nullptr, wqkv, wt);
    else k_r<1><<<256, 576, 0, stream>>>(part, xr, xn, nullptr, nullptr, nullptr);
    k_a<<<256, 512, A_LDS, stream>>>(xn, wt, pq, kvp);
    k_a2<<<216, 256, 0, stream>>>(kvp, wo, mt, zt);
    k_b<<<256, 576, 0, stream>>>(pq, mt, zt, xr, x1, st);
    k_c<<<256, 512, C_LDS, stream>>>(x1, st, wg, wu, h, dbg);
    k_d<<<256, 512, D_LDS, stream>>>(h, wd, x1, part, dbg ? dbg + 256 * 32 : dbg);
  }
  k_r<2><<<256, 576, 0, stream>>>(part, nullptr, nullptr, (u16*)d_out, nullptr, nullptr);
}
```

```cpp
#include <hip/hip_runtime.h>
#include <hip/hip_bf16.h>
#include <stdint.h>

#define LAS __attribute__((address_space(3)))
#define GAS __attribute__((address_space(1)))
#define DI __device__ __forceinline__
typedef __attribute__((ext_vector_type(8))) __bf16 bf16x8;
typedef __attribute__((ext_vector_type(4))) __bf16 bf16x4;
typedef __attribute__((ext_vector_type(2))) __bf16 bf16x2;
typedef __attribute__((ext_vector_type(4))) short s16x4;
typedef __attribute__((ext_vector_type(8))) short s16x8;
typedef __attribute__((ext_vector_type(4))) float f32x4;
typedef __attribute__((ext_vector_type(2))) float f32x2;
typedef __attribute__((ext_vector_type(4))) unsigned u32x4;
typedef __attribute__((ext_vector_type(2))) unsigned u32x2;
typedef unsigned short u16;

constexpr int S_ = 2048, D_ = 576, F_ = 4608;
#define MFMA16(a, b, c) __builtin_amdgcn_mfma_f32_16x16x32_bf16((a), (b), (c), 0, 0, 0)

DI float bf2f(u16 b) { return __uint_as_float((unsigned)b << 16); }
DI u16 f2bf(float f) { __bf16 b = (__bf16)f; return __builtin_bit_cast(u16, b); }
DI unsigned pk2(float a, float b) { f32x2 v = {a, b}; bf16x2 r = __builtin_convertvector(v, bf16x2); return __builtin_bit_cast(unsigned, r); }
DI float lo16(unsigned u) { return __uint_as_float(u << 16); }
DI float hi16(unsigned u) { return __uint_as_float(u & 0xffff0000u); }
__host__ __device__ __forceinline__ size_t hf(int t, int c, int C) { return (((size_t)(t >> 4) * (C >> 5) + (c >> 5)) * 64 + ((c >> 2) & 3) * 16 + (t & 15)) * 8 + ((c >> 4) & 1) * 4 + (c & 3); }

DI void glds16(unsigned voff, const void* sbase, unsigned lds) {
  asm volatile("s_mov_b32 m0, %2\n\ts_nop 0\n\tglobal_load_lds_dwordx4 %0, %1" :: "v"(voff), "s"(sbase), "s"(lds) : "memory");
}
#define WAITV(n) asm volatile("s_waitcnt vmcnt(%0)" :: "n"(n) : "memory")
#define WAITL0() asm volatile("s_waitcnt lgkmcnt(0)" ::: "memory")
#define BAR() do { asm volatile("" ::: "memory"); __builtin_amdgcn_s_barrier(); asm volatile("" ::: "memory"); } while (0)

DI bf16x8 ld_frag(LAS char* p) { return __builtin_bit_cast(bf16x8, *(LAS u32x4*)p); }
#define STAMP(i) do {} while (0)
#define TL_PARAM
#define TL_ARG(k)
#define TL_BEGIN() do {} while (0)
#define TL_END() do {} while (0)
#define DBG_PARAM
#define DBG_ARG(k)
#define RT_BEGIN() do {} while (0)
#define RT_END() do {} while (0)
#define SB0() __builtin_amdgcn_sched_barrier(0)
#define SGB(m, n) __builtin_amdgcn_sched_group_barrier((m), (n), 0)
DI bf16x8 ld_tr(LAS char* p0, LAS char* p1) {
  s16x4 a = __builtin_amdgcn_ds_read_tr16_b64_v4i16((LAS s16x4*)p0);
  s16x4 b = __builtin_amdgcn_ds_read_tr16_b64_v4i16((LAS s16x4*)p1);
  s16x8 r = __builtin_shufflevector(a, b, 0, 1, 2, 3, 4, 5, 6, 7);
  return __builtin_bit_cast(bf16x8, r);
}
DI float geglu1(float g, float u) {
  const float c1 = -2.0f * 0.7978845608028654f * 1.4426950408889634f;
  float p = __builtin_fmaf(g * g, c1 * 0.044715f, c1);
  float e = __builtin_amdgcn_exp2f(p * g);
  float r = __builtin_amdgcn_rcpf(1.0f + e);
  return g * u * r;
}

constexpr int XSLOT = 18432;
constexpr int C_LDS = 4 * XSLOT + 5 * 16384 + 304 * 16;
#define AMF(acc, a, b) asm volatile("v_mfma_f32_16x16x32_bf16 %0, %1, %2, %0" : "+v"(acc) : "v"(a), "v"(b))
#define AMF0(acc, a, b) asm volatile("v_mfma_f32_16x16x32_bf16 %0, %1, %2, 0" : "=&v"(acc) : "v"(a), "v"(b))
#define WAITLG(n) __builtin_amdgcn_s_waitcnt(0xC07F | ((n) << 8))
#define PIN(x) asm volatile("" : "+v"(x))
#define HST "sc1"
#define PSC1
DI void gstore8(unsigned voff, const void* sbase, u32x2 v) { asm volatile("global_store_dwordx2 %0, %1, %2 " HST :: "v"(voff), "v"(v), "s"(sbase) : "memory"); }
DI void pst16(void* p, u32x4 v) { asm volatile("global_store_dwordx4 %0, %1, off sc1\n\ts_nop 1" :: "v"(p), "v"(v) : "memory"); }
DI void pst8(void* p, u32x2 v) { asm volatile("global_store_dwordx2 %0, %1, off sc1" :: "v"(p), "v"(v) : "memory"); }
DI void pst4(void* p, float v) { asm volatile("global_store_dword %0, %1, off sc1" :: "v"(p), "v"(v) : "memory"); }
constexpr int cstep(int k) { return k > 17 ? 0 : 2 + ((k & 1) ? 0 : 1); }
constexpr int csum(int a, int b) { int r = 0; for (int k = a; k <= b; ++k) r += cstep(k); return r; }

__global__ __launch_bounds__(512, 2) void k_c(const u16* __restrict__ x1, const float* __restrict__ st, const u16* __restrict__ Wg,
                                              const u16* __restrict__ Wu, u16* __restrict__ h, unsigned long long* dbg TL_PARAM) {
  extern __shared__ __attribute__((aligned(16))) char smem_[];
  LAS char* smem = (LAS char*)smem_;
  const int tid = threadIdx.x, lane = tid & 63;
  const int w = __builtin_amdgcn_readfirstlane(tid >> 6);
  const int xcd = blockIdx.x & 7, idx = blockIdx.x >> 3;
  const int start = (63 * xcd + 1) >> 1, cnt = ((63 * (xcd + 1) + 1) >> 1) - start;
  if (idx >= cnt) return;
  TL_BEGIN();
  STAMP(0);
  const int L = start + idx, slice = L / 7, range = L % 7;
  const int ntile = range < 2 ? 19 : 18, tile0 = range < 2 ? 19 * range : 38 + 18 * (range - 2);
  const unsigned ldsb = (unsigned)(size_t)smem;
  const int g = lane >> 4, t16 = lane & 15;

  u32x4 s0, s1;
  { const unsigned so = (unsigned)min(tid, ntile * 16 - 1) * 32u; const float* sb = st + (size_t)tile0 * 16 * 8;
    asm volatile("global_load_dwordx4 %0, %1, %2" : "=v"(s0) : "v"(so), "s"(sb) : "memory");
    asm volatile("global_load_dwordx4 %0, %1, %2 offset:16" : "=v"(s1) : "v"(so), "s"(sb) : "memory"); }

  const char* xb = (const char*)x1 + (size_t)tile0 * XSLOT;
  const unsigned vlin = lane * 16;
  const int row32 = 4 * w + g;
  const unsigned wvoff = row32 * (F_ * 2) + ((t16 ^ (2 * (row32 & 7))) * 16);
  const char* wgb = (const char*)Wg + slice * 256;
  const char* wub = (const char*)Wu + slice * 256;
  constexpr int WR0 = 4 * XSLOT;
  auto wslot = [&](int k) -> int { return WR0 + ((k + 2) % 5) * 16384; };
  auto issueW = [&](int k, int mat) {
    glds16(wvoff, (mat ? wub : wgb) + (size_t)k * 32 * (F_ * 2), ldsb + wslot(k) + mat * 8192 + w * 1024);
  };
  auto issueX = [&](int s) {
    const int o = (w & 3) * XSLOT + (s + (w >> 2)) * 1024; glds16(vlin, xb + o, ldsb + o);
  };
  const unsigned vp0 = vlin + w * 1024, vp1 = vp0 + 8192, vp2 = vp0 + 16384;
  const unsigned ldw = ldsb + w * 1024;
  auto load_piece = [&](int T, int pc) {
    const int Tc = min(T, ntile - 1);
    const char* src = xb + Tc * XSLOT; const unsigned dst = ldw + (T & 7) * XSLOT + pc * 8192;
    if (pc < 2 || w < 2) glds16(pc == 0 ? vp0 : pc == 1 ? vp1 : vp2, src, dst);
  };
  constexpr int LA = 5;
#pragma unroll
  for (int s = 0; s < LA; ++s) { if ((s & 1) == 0) issueX(s); issueW(s, 0); issueW(s, 1); if (s < LA - 1) BAR(); }

  bf16x8 wg[18], wu[18];
  f32x4 a1g[4], a1u[4], cg, cu;
  const u32x4 ones_u = {0x3f803f80u, 0x3f803f80u, 0x3f803f80u, 0x3f803f80u};
  bf16x8 ones = __builtin_bit_cast(bf16x8, ones_u);
  PIN(ones);
  const int q = t16 >> 2, p = lane & 3, rrow = 4 * g + q;
  const unsigned tr_off = rrow * 256 + (((2 * w + (p >> 1)) ^ (2 * (rrow & 7))) * 16) + 8 * (p & 1);
  LAS char* xl = smem + lane * 16;
  LAS char* wtr = smem + tr_off;
  bf16x8 xf1[2][4];
  constexpr int STATS = WR0 + 5 * 16384;
  {
    asm volatile("s_waitcnt vmcnt(%2)" : "+v"(s0), "+v"(s1) : "n"(csum(2, LA - 1)) : "memory");
    STAMP(1);
    float sum = __uint_as_float(s0[0]) + __uint_as_float(s0[2]) + __uint_as_float(s1[0]) + __uint_as_float(s1[2]);
    float sq = __uint_as_float(s0[1]) + __uint_as_float(s0[3]) + __uint_as_float(s1[1]) + __uint_as_float(s1[3]);
    float mean = sum * (1.0f / D_), var = sq * (1.0f / D_) - mean * mean;
    const float irs2 = fmaxf(var, 0.f) + 1e-5f, rs = __builtin_amdgcn_rsqf(irs2);
    const float c1 = -2.0f * 0.7978845608028654f * 1.4426950408889634f;
    if (tid < 304) *(LAS f32x4*)(smem + STATS + tid * 16) = f32x4{-mean, c1 * rs, c1 * 0.044715f * rs * rs * rs, irs2};
    WAITL0(); BAR();
    wg[0] = ld_tr(wtr + wslot(0), wtr + wslot(0) + 4096);
    wu[0] = ld_tr(wtr + wslot(0) + 8192, wtr + wslot(0) + 8192 + 4096);
#pragma unroll
    for (int T = 0; T < 4; ++T) xf1[0][T] = ld_frag(xl + T * XSLOT);
  }
#define P1MF(acc, W_, X_) do { if (s == 0) AMF0(acc, W_, X_); else AMF(acc, W_, X_); } while (0)
  const int half = w >> 2;
  auto vop = [&](int s, int n) {
    const int k = s + LA;
    if (k < 18) {
      if (n == 0) { if ((k & 1) == 0) issueX(k); }
      else if (n == 1) issueW(k, 0);
      else issueW(k, 1);
    } else {
      if (s >= 14) load_piece(s - 10, n);
    }
  };
#pragma unroll
  for (int s = 0; s < 18; ++s) {
    SB0();
    P1MF(a1g[0], wg[s], xf1[s & 1][0]);
    if (s < 17) wg[s + 1] = ld_tr(wtr + wslot(s + 1), wtr + wslot(s + 1) + 4096);
    if (half == 0) vop(s, 0);
    SB0();
    P1MF(a1u[0], wu[s], xf1[s & 1][0]);
    if (s < 17) wu[s + 1] = ld_tr(wtr + wslot(s + 1) + 8192, wtr + wslot(s + 1) + 8192 + 4096);
    SB0();
    P1MF(a1g[1], wg[s], xf1[s & 1][1]);
    if (s < 17) xf1[(s + 1) & 1][0] = ld_frag(xl + 0 * XSLOT + (s + 1) * 1024);
    if (half == 1) vop(s, 0);
    SB0();
    P1MF(a1u[1], wu[s], xf1[s & 1][1]);
    if (s < 17) xf1[(s + 1) & 1][1] = ld_frag(xl + 1 * XSLOT + (s + 1) * 1024);
    if (half == 0) vop(s, 1);
    SB0();
    P1MF(a1g[2], wg[s], xf1[s & 1][2]);
    if (s < 17) xf1[(s + 1) & 1][2] = ld_frag(xl + 2 * XSLOT + (s + 1) * 1024);
    SB0();
    P1MF(a1u[2], wu[s], xf1[s & 1][2]);
    if (s < 17) xf1[(s + 1) & 1][3] = ld_frag(xl + 3 * XSLOT + (s + 1) * 1024);
    if (half == 1) vop(s, 1);
    SB0();
    P1MF(a1g[3], wg[s], xf1[s & 1][3]);
    if (half == 0) vop(s, 2);
    SB0();
    P1MF(a1u[3], wu[s], xf1[s & 1][3]);
    SB0();
    P1MF(cg, wg[s], ones);
    if (half == 1) vop(s, 2);
    SB0();
    P1MF(cu, wu[s], ones);
    SB0();
    if (s < 17) {
      if (s + 2 < 18) {
        switch (s) {
#define WCASE(S) case S: WAITV(csum(S + 3, (S + LA < 17 ? S + LA : 17)) + (S >= 14 ? (S - 13) * 2 : 0)); break;
          WCASE(0) WCASE(1) WCASE(2) WCASE(3) WCASE(4) WCASE(5) WCASE(6) WCASE(7) WCASE(8) WCASE(9) WCASE(10) WCASE(11) WCASE(12) WCASE(13) WCASE(14) WCASE(15)
#undef WCASE
        }
      }
      WAITL0(); BAR();
    }
  }
  STAMP(2);
  WAITV(6); WAITL0(); BAR();

  const char* hb = (const char*)h + ((size_t)tile0 * (F_ / 32) + slice * 4 + (w >> 1)) * 1024;
  const unsigned hvo = lane * 16 + (w & 1) * 8;
  constexpr size_t HT = (size_t)(F_ / 32) * 1024;
  auto epi = [&](const f32x4& ag, const f32x4& au, int T) -> u32x2 {
    f32x4 sv = *(LAS f32x4*)(smem + STATS + (T * 16 + t16) * 16);
    float hv[4];
#pragma unroll
    for (int i = 0; i < 4; ++i) {
      float tg = __builtin_fmaf(sv[0], cg[i], ag[i]), tu = __builtin_fmaf(sv[0], cu[i], au[i]);
      float y = __builtin_fmaf(tg * tg, sv[2], sv[1]) * tg;
      float e = __builtin_amdgcn_exp2f(y);
      float r = __builtin_amdgcn_rcpf(__builtin_fmaf(e, sv[3], sv[3]));
      hv[i] = tg * tu * r;
    }
    return u32x2{pk2(hv[0], hv[1]), pk2(hv[2], hv[3])};
  };
  constexpr int PD = 4;
  bf16x8 xr[6];
#pragma unroll
  for (int s = 0; s < PD; ++s) xr[s] = ld_frag(xl + 4 * XSLOT + s * 1024);
  asm volatile("s_nop 7\n\ts_nop 1" : "+v"(a1g[0]), "+v"(a1g[1]), "+v"(a1g[2]), "+v"(a1g[3]), "+v"(a1u[0]), "+v"(a1u[1]), "+v"(a1u[2]), "+v"(a1u[3]), "+v"(cg), "+v"(cu));
#pragma unroll
  for (int T = 0; T < 3; ++T) { u32x2 o = epi(a1g[T], a1u[T], T); gstore8(hvo, hb + (size_t)T * HT, o); }
  STAMP(3);
  const int ph = half;
  auto tile = [&](int T, f32x4& ag, f32x4& au, const f32x4& pg, const f32x4& pu) {
    LAS char* xt = xl + (T & 7) * XSLOT;
    LAS char* xn = xl + ((T + 1) & 7) * XSLOT;
    f32x4 sv = *(LAS f32x4*)(smem + STATS + ((T - 1) * 16 + t16) * 16);
    float tg[2], tu[2], y[2], e[2], m[2], r[2];
    unsigned o[2] = {0, 0};
    const char* nsrc = xb + min(T + 4, ntile - 1) * XSLOT; const unsigned ndst = ldw + ((T + 4) & 7) * XSLOT;
    auto eslot = [&](int q) {
      if (q < 0 || q >= 24) return;
      const int pr = q / 12, op = q % 12, ia = 2 * pr, ib = 2 * pr + 1;
#define SEP(x)
      switch (op) {
        case 0: tg[0] = __builtin_fmaf(sv[0], cg[ia], pg[ia]); SEP(tg[0]); tg[1] = __builtin_fmaf(sv[0], cg[ib], pg[ib]); PIN(tg[0]); PIN(tg[1]); break;
        case 1: tu[0] = __builtin_fmaf(sv[0], cu[ia], pu[ia]); SEP(tu[0]); tu[1] = __builtin_fmaf(sv[0], cu[ib], pu[ib]); PIN(tu[0]); PIN(tu[1]); break;
        case 2: y[0] = tg[0] * tg[0]; SEP(y[0]); y[1] = tg[1] * tg[1]; PIN(y[0]); PIN(y[1]); break;
        case 3: y[0] = __builtin_fmaf(y[0], sv[2], sv[1]); SEP(y[0]); y[1] = __builtin_fmaf(y[1], sv[2], sv[1]); PIN(y[0]); PIN(y[1]); break;
        case 4: y[0] = y[0] * tg[0]; SEP(y[0]); y[1] = y[1] * tg[1]; PIN(y[0]); PIN(y[1]); break;
        case 5: e[0] = __builtin_amdgcn_exp2f(y[0]); m[0] = tg[0] * tu[0]; PIN(e[0]); PIN(m[0]); break;
        case 6: e[1] = __builtin_amdgcn_exp2f(y[1]); m[1] = tg[1] * tu[1]; PIN(e[1]); PIN(m[1]); break;
        case 7: e[0] = __builtin_fmaf(e[0], sv[3], sv[3]); SEP(e[0]); e[1] = __builtin_fmaf(e[1], sv[3], sv[3]); PIN(e[0]); PIN(e[1]); break;
        case 8: r[0] = __builtin_amdgcn_rcpf(e[0]); PIN(r[0]); break;
        case 9: r[1] = __builtin_amdgcn_rcpf(e[1]); PIN(r[1]); break;
        case 10: m[0] = m[0] * r[0]; SEP(m[0]); m[1] = m[1] * r[1]; PIN(m[0]); PIN(m[1]); break;
        default: o[pr] = pk2(m[0], m[1]); PIN(o[pr]); break;
      }
    };
    SB0();
#pragma unroll
    for (int s = 0; s < 18; ++s) {
      if (s == 8) {
        WAITV(4);
        BAR();
        SB0();
      }
      if (s == 0) AMF0(ag, wg[0], xr[0]); else AMF(ag, wg[s], xr[s % 6]);
      SB0();
      if (s + PD < 18) xr[(s + PD) % 6] = ld_frag(xt + (s + PD) * 1024); else xr[(s + PD) % 6] = ld_frag(xn + (s + PD - 18) * 1024);
      eslot(2 * s - 4);
      SB0();
      if (s == 0) AMF0(au, wu[0], xr[0]); else AMF(au, wu[s], xr[s % 6]);
      SB0();
      eslot(2 * s - 3);
      if (s >= 9 && s <= 14) { const int pc = (s - 9) >> 1; if (((s - 9) & 1) == ph && (pc < 2 || w < 2)) glds16(pc == 0 ? vp0 : pc == 1 ? vp1 : vp2, nsrc, ndst + pc * 8192); }
      if (s == 15) gstore8(hvo, hb + (size_t)(T - 1) * HT, u32x2{o[0], o[1]});
      SB0();
    }
  };
  f32x4 Ag, Au, Bg, Bu;
  int T = 4;
  if (ntile == 19) { tile(4, Bg, Bu, a1g[3], a1u[3]); asm volatile("s_nop 7\n\ts_nop 1" : "+v"(Bg), "+v"(Bu)); T = 5; } else { Bg = a1g[3]; Bu = a1u[3]; }
  for (; T < ntile; T += 2) {
    tile(T, Ag, Au, Bg, Bu);
    tile(T + 1, Bg, Bu, Ag, Au);
  }
  STAMP(4);
  asm volatile("s_nop 7\n\ts_nop 1" : "+v"(Bg), "+v"(Bu));
  { u32x2 o = epi(Bg, Bu, ntile - 1); gstore8(hvo, hb + (size_t)(ntile - 1) * HT, o); }
  STAMP(5);
  TL_END();
}

constexpr int D_STAGE = 2 * 2 * 12288;
constexpr int D_LDS = 3 * D_STAGE;
__global__ __launch_bounds__(512, 2) void k_d(const u16* __restrict__ h, const u16* __restrict__ Wd, const u16* __restrict__ x1, u16* __restrict__ part, unsigned long long* dbg TL_PARAM) {
  TL_BEGIN();
  extern __shared__ __attribute__((aligned(16))) char smem_[];
  LAS char* smem = (LAS char*)smem_;
  const int tid = threadIdx.x, lane = tid & 63;
  const int w = __builtin_amdgcn_readfirstlane(tid >> 6);
  const int xcd = blockIdx.x & 7, idx = blockIdx.x >> 3;
  const int ks = xcd & 3, tt = (xcd >> 2) * 8 + (idx >> 2), nt = idx & 3;
  const int mq = w & 3, kh = w >> 2;
  const unsigned ldsb = (unsigned)(size_t)smem;
  const int g = lane >> 4, t16 = lane & 15;
  STAMP(0);
  unsigned wv[3];
#pragma unroll
  for (int i = 0; i < 3; ++i) { int qq = 64 * i + lane, k = qq / 24, pp = qq % 24; int c = (pp & ~7) | ((pp & 7) ^ (2 * ((k >> 1) & 3))); wv[i] = k * (D_ * 2) + c * 16; }
  const int krow0 = ks * 1152 + kh * 576;
  const char* wb = (const char*)Wd + (size_t)(krow0 + 8 * mq) * (D_ * 2) + nt * 256;
  const int inner = nt * 32;
  const int rot = 0;
  auto iof = [&](int it) { int k = it + rot; return k >= 9 ? k - 9 : k; };
  auto stageW1 = [&](int it, int n) {
    const int u = n / 3, i = n % 3;
    const unsigned dst = ldsb + (it % 3) * D_STAGE + kh * 24576 + mq * 3072;
    glds16(wv[i], wb + (size_t)(iof(it) * 2 + u) * 32 * (D_ * 2), dst + u * 12288 + i * 1024);
  };
  const char* hb = (const char*)h + ((size_t)(tt * 8 + 2 * mq) * (F_ / 32) + krow0 / 32) * 1024;
  const unsigned hv = lane * 16;
  u32x4 hr[3][4];
  auto loadH1 = [&](int it, int n, u32x4& r) {
    const int u = n >> 1, m = n & 1;
    asm volatile("global_load_dwordx4 %0, %1, %2" : "=v"(r) : "v"(hv), "s"(hb + (size_t)m * (F_ / 32) * 1024 + (iof(it) * 2 + u) * 1024) : "memory");
  };
  stageW1(0, 0); stageW1(0, 1); stageW1(0, 2); loadH1(0, 0, hr[0][0]); loadH1(0, 1, hr[0][1]);
  BAR();
  stageW1(0, 3); stageW1(0, 4); stageW1(0, 5); loadH1(0, 2, hr[0][2]); loadH1(0, 3, hr[0][3]);
  BAR();
#pragma unroll
  for (int n = 0; n < 6; ++n) stageW1(1, n);
#pragma unroll
  for (int n = 0; n < 4; ++n) loadH1(1, n, hr[1][n]);
  stageW1(2, 0);
  f32x4 acc[2][9];
  const int q = t16 >> 2, p = lane & 3, rr = 4 * g + q;
  const int sw = 2 * ((rr >> 1) & 3);
  LAS char* wl = smem + kh * 24576 + rr * 384 + 8 * (p & 1);
  unsigned poff[9];
#pragma unroll
  for (int j = 0; j < 9; ++j) { const int c = inner / 16 + 2 * j + (p >> 1); poff[j] = ((c & ~7) | ((c & 7) ^ sw)) * 16; }
  constexpr int WPD = 3;
  bf16x8 wf[6];
  auto rdW = [&](int it, int i) -> bf16x8 { const int u = i / 9, j = i % 9; LAS char* a = wl + (it % 3) * D_STAGE + u * 12288 + poff[j]; return ld_tr(a, a + 16 * 384); };
  asm volatile("s_waitcnt vmcnt(16)" : "+v"(hr[0][0]), "+v"(hr[0][1]) :: "memory");
  STAMP(1);
  BAR();
#pragma unroll
  for (int i = 0; i < WPD; ++i) wf[i] = rdW(0, i);
#pragma unroll
  for (int it = 0; it < 9; ++it) {
    u32x4(&r)[4] = hr[it % 3];
    SB0();
#pragma unroll
    for (int i = 0; i < 18; ++i) {
      const int u = i / 9, j = i % 9;
      if (it == 0 && i == 9 - WPD) {
        asm volatile("s_waitcnt vmcnt(14)" : "+v"(hr[0][2]), "+v"(hr[0][3]) :: "memory");
        BAR();
        SB0();
      }
      if (i == 18 - WPD && it < 8) {
        u32x4(&rn)[4] = hr[(it + 1) % 3];
        if (it < 7) asm volatile("s_waitcnt vmcnt(9) lgkmcnt(0)" : "+v"(rn[0]), "+v"(rn[1]), "+v"(rn[2]), "+v"(rn[3]) :: "memory");
        else asm volatile("s_waitcnt vmcnt(0) lgkmcnt(0)" : "+v"(rn[0]), "+v"(rn[1]), "+v"(rn[2]), "+v"(rn[3]) :: "memory");
        BAR();
        SB0();
      }
      if (i + WPD < 18) wf[(i + WPD) % 6] = rdW(it, i + WPD);
      else if (it < 8) wf[(i + WPD) % 6] = rdW(it + 1, i + WPD - 18);
      if (it == 0 && u == 0) { AMF0(acc[0][j], wf[i % 6], r[u * 2 + 0]); AMF0(acc[1][j], wf[i % 6], r[u * 2 + 1]); }
      else { AMF(acc[0][j], wf[i % 6], r[u * 2 + 0]); AMF(acc[1][j], wf[i % 6], r[u * 2 + 1]); }
      if (it + 2 < 9) {
        if (i <= 8 && (i & 1) == 0) { if (i > 0 || true) { const int n = 1 + i / 2; if (n <= 5) stageW1(it + 2, n); } }
        if (i == 10 || i == 12 || i == 14) loadH1(it + 2, (i - 10) / 2, hr[(it + 2) % 3][(i - 10) / 2]);
        if (i == 16) loadH1(it + 2, 3, hr[(it + 2) % 3][3]);
      }
      if (i == 17 && it + 3 < 9) stageW1(it + 3, 0);
      SB0();
    }
  }
  STAMP(2);
  asm volatile("s_nop 7\n\ts_nop 1" ::: "memory");
  const int Tg = tt * 8 + 2 * mq + kh;
  const u16* xrow = x1 + ((size_t)Tg * 18 * 64 + lane) * 8;
  u32x2 xres[9];
#pragma unroll
  for (int j = 0; j < 9; ++j) xres[j] = u32x2{0, 0};
  if (ks == 0) {
#pragma unroll
    for (int j = 0; j < 9; ++j) { const int cb = nt * 9 + j; xres[j] = *(const GAS u32x2*)(xrow + (cb >> 1) * 512 + (cb & 1) * 4); }
  }
  LAS f32x4* red = (LAS f32x4*)smem;
  u32x2 o[9];
  auto fin = [&](f32x4 (&keep)[9], f32x4 (&send)[9]) {
#pragma unroll
    for (int j = 0; j < 9; ++j) red[(w * 9 + j) * 64 + lane] = send[j];
    WAITL0(); BAR();
#pragma unroll
    for (int j = 0; j < 9; ++j) {
      f32x4 v = keep[j] + red[((w ^ 4) * 9 + j) * 64 + lane];
      v[0] += lo16(xres[j][0]); v[1] += hi16(xres[j][0]); v[2] += lo16(xres[j][1]); v[3] += hi16(xres[j][1]);
      o[j] = u32x2{pk2(v[0], v[1]), pk2(v[2], v[3])};
    }
  };
  if (kh == 0) fin(acc[0], acc[1]); else fin(acc[1], acc[0]);
  u16* prow = part + (size_t)ks * S_ * D_ + ((size_t)Tg * 18 * 64 + lane) * 8;
  const int cb0 = nt * 9;
  if ((nt & 1) == 0) {
#pragma unroll
    for (int j = 0; j < 8; j += 2) pst16(prow + ((cb0 + j) >> 1) * 512, u32x4{o[j][0], o[j][1], o[j + 1][0], o[j + 1][1]});
    pst8(prow + ((cb0 + 8) >> 1) * 512, o[8]);
  } else {
    pst8(prow + (cb0 >> 1) * 512 + 4, o[0]);
#pragma unroll
    for (int j = 1; j < 9; j += 2) pst16(prow + ((cb0 + j) >> 1) * 512, u32x4{o[j][0], o[j][1], o[j + 1][0], o[j + 1][1]});
  }
  STAMP(3);
  TL_END();
}

template <int MODE>
__global__ __launch_bounds__(576) void k_r(const u16* __restrict__ src, u16* __restrict__ xr, u16* __restrict__ xn, u16* __restrict__ outp,
                                           const u16* __restrict__ wqkv, u16* __restrict__ wt TL_PARAM) {
  TL_BEGIN();
  const int tid = threadIdx.x, lane = tid & 63, w = tid >> 6;
  if (MODE == 0 && blockIdx.x >= 256) {
    const int f = (blockIdx.x - 256) * 9 + w;
    if (f < 16 * 7 * 18) {
      const int s = f % 18, j = (f / 18) % 7, hp = f / 126;
      const int n16 = lane & 15, g = lane >> 4;
      int col; bool valid = true;
      if (j < 6) col = (j >> 1) * 576 + 36 * hp + (j & 1) * 16 + n16;
      else { const int sec = n16 >> 2; valid = sec < 3; col = (valid ? sec : 0) * 576 + 36 * hp + 32 + (n16 & 3); }
      unsigned v[8];
#pragma unroll
      for (int e = 0; e < 8; ++e) { const int k = 32 * s + 16 * (e >> 2) + 4 * g + (e & 3); v[e] = valid ? wqkv[(size_t)k * 1728 + col] : 0; }
      u32x4 o = {v[0] | (v[1] << 16), v[2] | (v[3] << 16), v[4] | (v[5] << 16), v[6] | (v[7] << 16)};
      pst16(wt + ((size_t)f * 64 + lane) * 8, o);
    }
    TL_END();
    return;
  }
  __shared__ float red[9][8][2];
  const int t8 = tid & 7, cg = tid >> 3, c32 = cg >> 2, g = cg & 3;
  const int t = blockIdx.x * 8 + t8;
  const size_t ffo = (((size_t)(t >> 4) * 18 + c32) * 64 + g * 16 + (t & 15)) * 8;
  float v[8];
  if (MODE == 0) {
    const u32x2 a = *(const u32x2*)(src + (size_t)t * D_ + 32 * c32 + 4 * g), b = *(const u32x2*)(src + (size_t)t * D_ + 32 * c32 + 16 + 4 * g);
    v[0] = lo16(a[0]); v[1] = hi16(a[0]); v[2] = lo16(a[1]); v[3] = hi16(a[1]); v[4] = lo16(b[0]); v[5] = hi16(b[0]); v[6] = lo16(b[1]); v[7] = hi16(b[1]);
  } else {
    u32x4 p[4];
#pragma unroll
    for (int i = 0; i < 4; ++i) p[i] = *(const u32x4*)(src + (size_t)i * S_ * D_ + ffo);
#pragma unroll
    for (int e = 0; e < 4; ++e) {
      v[2 * e] = (lo16(p[0][e]) + lo16(p[1][e])) + (lo16(p[2][e]) + lo16(p[3][e]));
      v[2 * e + 1] = (hi16(p[0][e]) + hi16(p[1][e])) + (hi16(p[2][e]) + hi16(p[3][e]));
    }
  }
  u32x4 xb = {pk2(v[0], v[1]), pk2(v[2], v[3]), pk2(v[4], v[5]), pk2(v[6], v[7])};
  if (MODE == 2) {
    *(u32x2*)(outp + (size_t)t * D_ + 32 * c32 + 4 * g) = u32x2{xb[0], xb[1]};
    *(u32x2*)(outp + (size_t)t * D_ + 32 * c32 + 16 + 4 * g) = u32x2{xb[2], xb[3]};
    TL_END();
    return;
  }
  pst16(xr + ffo, xb);
#pragma unroll
  for (int e = 0; e < 4; ++e) { v[2 * e] = lo16(xb[e]); v[2 * e + 1] = hi16(xb[e]); }
  float m = ((v[0] + v[1]) + (v[2] + v[3]) + (v[4] + v[5]) + (v[6] + v[7])) * 0.125f, M2 = 0.f;
#pragma unroll
  for (int e = 0; e < 8; ++e) { const float d = v[e] - m; M2 = __builtin_fmaf(d, d, M2); }
  float n = 8.f;
#pragma unroll
  for (int o = 8; o < 64; o <<= 1) {
    const float m2 = __shfl_xor(m, o), q2 = __shfl_xor(M2, o), d = m - m2;
    M2 = M2 + q2 + d * d * (n * 0.5f); m = 0.5f * (m + m2); n *= 2.f;
  }
  if (lane < 8) { red[w][lane][0] = m; red[w][lane][1] = M2; }
  __syncthreads();
  float mt = 0.f;
#pragma unroll
  for (int i = 0; i < 9; ++i) mt += red[i][t8][0];
  mt *= (1.0f / 9.0f);
  float Mt = 0.f;
#pragma unroll
  for (int i = 0; i < 9; ++i) { const float d = red[i][t8][0] - mt; Mt += red[i][t8][1] + d * d * 64.f; }
  const float rs = 1.0f / sqrtf(Mt * (1.0f / D_) + 1e-5f);
  u32x4 o = {pk2((v[0] - mt) * rs, (v[1] - mt) * rs), pk2((v[2] - mt) * rs, (v[3] - mt) * rs), pk2((v[4] - mt) * rs, (v[5] - mt) * rs), pk2((v[6] - mt) * rs, (v[7] - mt) * rs)};
  pst16(xn + ffo, o);
  TL_END();
}

constexpr int A_STAGE = 32768;
constexpr int A_LDS = 4 * A_STAGE;
DI float phi(float t) { return (t > 0.f ? t + 1.0f : __builtin_amdgcn_exp2f(t * 1.4426950408889634f)) + 1e-6f; }
__global__ __launch_bounds__(512, 2) void k_a(const u16* __restrict__ xn, const u16* __restrict__ wt, u16* __restrict__ pq, float* __restrict__ kvp TL_PARAM DBG_PARAM) {
  TL_BEGIN();
  extern __shared__ __attribute__((aligned(16))) char smem_[];
  LAS char* smem = (LAS char*)smem_;
  const int tid = threadIdx.x, lane = tid & 63;
  const int w = __builtin_amdgcn_readfirstlane(tid >> 6);
  const int xcd = blockIdx.x & 7, idx = blockIdx.x >> 3;
  const int tt = (xcd >> 1) * 4 + (idx >> 3), hp = (xcd & 1) * 8 + (idx & 7);
  const unsigned ldsb = (unsigned)(size_t)smem;
  const int g = lane >> 4, t16 = lane & 15;
  const unsigned vlin = lane * 16;
  RT_BEGIN(); STAMP(0);
  const char* xs = (const char*)xn + (size_t)(tt * 8 + w) * 18432;
  const char* wsrc = (const char*)wt + (size_t)(hp * 7 + min(w, 6)) * 18432;
  auto stage1 = [&](int st, int n) {
    const int u = n >> 1;
    const unsigned dst = ldsb + (st & 3) * A_STAGE + w * 1024 + u * 16384;
    if ((n & 1) == 0) glds16(vlin, xs + (2 * st + u) * 1024, dst); else glds16(vlin, wsrc + (2 * st + u) * 1024, dst + 8192);
  };
  stage1(0, 0); stage1(0, 1); BAR(); stage1(0, 2); stage1(0, 3); BAR();
  stage1(1, 0); stage1(1, 1); stage1(1, 2); stage1(1, 3); BAR();
  stage1(2, 0); stage1(2, 1); stage1(2, 2); stage1(2, 3);
  const int tp = w & 3, cgp = w >> 2;
  f32x4 acc[2][4];
#pragma unroll
  for (int m = 0; m < 2; ++m)
#pragma unroll
    for (int j = 0; j < 4; ++j) acc[m][j] = f32x4{0, 0, 0, 0};
  LAS char* xl = smem + lane * 16 + tp * 2048;
  LAS char* wl = smem + lane * 16 + 8192 + cgp * 4096;
#pragma unroll
  for (int st = 0; st < 9; ++st) {
    if (st < 7) WAITV(8); else if (st == 7) WAITV(4); else WAITV(0);
    if (st == 0) STAMP(1);
    WAITL0(); BAR();
#pragma unroll
    for (int u = 0; u < 2; ++u) {
      const int o = (st & 3) * A_STAGE + u * 16384;
      bf16x8 x0 = ld_frag(xl + o), x1 = ld_frag(xl + o + 1024);
      bf16x8 w0 = ld_frag(wl + o), w1 = ld_frag(wl + o + 1024), w2 = ld_frag(wl + o + 2048), w3 = ld_frag(wl + o + (cgp == 0 ? 3072 : 2048));
      SB0();
      if (st + 3 < 9) stage1(st + 3, 2 * u);
      SB0();
      acc[0][0] = MFMA16(w0, x0, acc[0][0]); acc[1][0] = MFMA16(w0, x1, acc[1][0]);
      acc[0][1] = MFMA16(w1, x0, acc[0][1]); acc[1][1] = MFMA16(w1, x1, acc[1][1]);
      SB0();
      if (st + 3 < 9) stage1(st + 3, 2 * u + 1);
      SB0();
      acc[0][2] = MFMA16(w2, x0, acc[0][2]); acc[1][2] = MFMA16(w2, x1, acc[1][2]);
      if (cgp == 0) { acc[0][3] = MFMA16(w3, x0, acc[0][3]); acc[1][3] = MFMA16(w3, x1, acc[1][3]); }
      SB0();
    }
  }
  STAMP(2);
  WAITL0(); BAR();
  constexpr int KVS = 224;
  if (tid < 128) {
    LAS char* r = smem + tid * KVS;
    *(LAS u32x2*)(r + 72) = u32x2{0, 0}; *(LAS u32x2*)(r + 80) = u32x2{0, 0}; *(LAS u32x2*)(r + 88) = u32x2{0, 0};
    *(LAS u32x2*)(r + 168) = u32x2{0x3f80u, 0}; *(LAS u32x2*)(r + 176) = u32x2{0, 0}; *(LAS u32x2*)(r + 184) = u32x2{0, 0};
  }
#pragma unroll
  for (int m = 0; m < 2; ++m) {
    const int tl = (2 * tp + m) * 16 + t16;
    LAS char* row = smem + tl * KVS;
    const size_t Tg = (size_t)(tt * 8 + 2 * tp + m);
    if (cgp == 0) {
      const f32x4 a = acc[m][0], b = acc[m][1];
      pst16(pq + ((Tg * 18 + hp) * 64 + lane) * 8, u32x4{pk2(phi(a[0]), phi(a[1])), pk2(phi(a[2]), phi(a[3])), pk2(phi(b[0]), phi(b[1])), pk2(phi(b[2]), phi(b[3]))});
      *(LAS u32x2*)(row + 8 * g) = u32x2{pk2(phi(acc[m][2][0]), phi(acc[m][2][1])), pk2(phi(acc[m][2][2]), phi(acc[m][2][3]))};
      *(LAS u32x2*)(row + 32 + 8 * g) = u32x2{pk2(phi(acc[m][3][0]), phi(acc[m][3][1])), pk2(phi(acc[m][3][2]), phi(acc[m][3][3]))};
    } else {
      *(LAS u32x2*)(row + 96 + 8 * g) = u32x2{pk2(acc[m][0][0], acc[m][0][1]), pk2(acc[m][0][2], acc[m][0][3])};
      *(LAS u32x2*)(row + 96 + 32 + 8 * g) = u32x2{pk2(acc[m][1][0], acc[m][1][1]), pk2(acc[m][1][2], acc[m][1][3])};
      const f32x4 a = acc[m][2];
      const u32x2 pv = u32x2{pk2(phi(a[0]), phi(a[1])), pk2(phi(a[2]), phi(a[3]))};
      if (g == 0) pst8(pq + ((Tg * 18 + 16 + (hp >> 3)) * 64 + ((hp >> 1) & 3) * 16 + t16) * 8 + (hp & 1) * 4, pv);
      else if (g == 1) *(LAS u32x2*)(row + 64) = pv;
      else if (g == 2) *(LAS u32x2*)(row + 96 + 64) = u32x2{pk2(a[0], a[1]), pk2(a[2], a[3])};
    }
  }
  STAMP(3);
  WAITL0(); BAR();
  const int kt = (0x02211100 >> (4 * w)) & 3, vt = (0x22121010 >> (4 * w)) & 3;
  f32x4 kv = {0, 0, 0, 0};
  const int q = t16 >> 2, p = lane & 3;
#pragma unroll
  for (int s = 0; s < 4; ++s) {
    LAS char* r0 = smem + (32 * s + 4 * g + q) * KVS + 8 * p;
    bf16x8 a = ld_tr(r0 + kt * 32, r0 + 16 * KVS + kt * 32);
    bf16x8 b = ld_tr(r0 + 96 + vt * 32, r0 + 16 * KVS + 96 + vt * 32);
    kv = MFMA16(a, b, kv);
  }
  STAMP(4);
  {
    LAS float* stg = (LAS float*)(smem + 32768);
    const int e = 16 * vt + t16;
#pragma unroll
    for (int i = 0; i < 4; ++i) {
      const int d = 16 * kt + 4 * g + i;
      if (d < 36) {
        const int hd = d >= 18 ? 1 : 0, dd = d - 18 * hd;
        if (e < 36) { const int he = e >= 18 ? 1 : 0; if (he == hd) stg[hd * 344 + dd * 18 + (e - 18 * he)] = kv[i]; }
        else if (e == 36) stg[hd * 344 + 324 + dd] = kv[i];
      }
    }
    if (tid < 4) stg[(tid >> 1) * 344 + 342 + (tid & 1)] = 0.f;
    WAITL0(); BAR();
    if (tid < 172) pst16(kvp + ((size_t)tt * 32 + 2 * hp) * 344 + tid * 4, *(LAS u32x4*)(stg + tid * 4));
  }
  STAMP(5);
  RT_END();
  TL_END();
}

constexpr int KVR = 344;
__global__ __launch_bounds__(512) void k_a2(const float* __restrict__ kvp, const u16* __restrict__ wo, u16* __restrict__ mt, u16* __restrict__ zt TL_PARAM) {
  TL_BEGIN();
  __shared__ __attribute__((aligned(16))) float kvs[2][KVR];
  __shared__ __attribute__((aligned(16))) u16 wos[36][48];
  const int tid = threadIdx.x, lane = tid & 63, w = tid >> 6;
  const int hp = blockIdx.x & 15, cgp = blockIdx.x >> 4;
  u32x4 wv = {0, 0, 0, 0};
  const int wi = tid - 256;
  if (wi >= 0 && wi < 216) wv = *(const u32x4*)(wo + (size_t)(36 * hp + wi / 6) * D_ + cgp * 48 + (wi % 6) * 8);
  if (tid < 172) {
    const f32x4* src = (const f32x4*)(kvp + (size_t)2 * hp * KVR) + tid;
    f32x4 v[16];
#pragma unroll
    for (int p = 0; p < 16; ++p) v[p] = src[(size_t)p * 32 * (KVR / 4)];
    f32x4 a = ((v[0] + v[1]) + (v[2] + v[3])) + ((v[4] + v[5]) + (v[6] + v[7])) + (((v[8] + v[9]) + (v[10] + v[11])) + ((v[12] + v[13]) + (v[14] + v[15])));
#pragma unroll
    for (int i = 0; i < 4; ++i) a[i] = bf2f(f2bf(a[i]));
    *(f32x4*)(&kvs[0][0] + tid * 4) = a;
  }
  if (wi >= 0 && wi < 216) *(u32x4*)&wos[wi / 6][(wi % 6) * 8] = wv;
  __syncthreads();
  const int n16 = lane & 15, g = lane >> 4;
  auto mrow4 = [&](int c0, int ct) -> u32x2 {
    float o[4];
#pragma unroll
    for (int i = 0; i < 4; ++i) {
      const int c = c0 + i, hh = c >= 18 ? 1 : 0, d = c - 18 * hh;
      const f32x2* kr = (const f32x2*)&kvs[hh][d * 18];
      float a = 0.f;
#pragma unroll
      for (int e = 0; e < 9; ++e) {
        const f32x2 kk = kr[e];
        a = __builtin_fmaf(kk[0], bf2f(wos[hh * 18 + 2 * e][ct * 16 + n16]), a);
        a = __builtin_fmaf(kk[1], bf2f(wos[hh * 18 + 2 * e + 1][ct * 16 + n16]), a);
      }
      o[i] = a;
    }
    return u32x2{pk2(o[0], o[1]), pk2(o[2], o[3])};
  };
  if (w < 6) {
    const int ct = w % 3, hf_ = w / 3;
    const int jglob = cgp * 3 + ct;
    pst8(mt + (((size_t)jglob * 18 + hp) * 64 + lane) * 8 + hf_ * 4, mrow4(16 * hf_ + 4 * g, ct));
  } else if (w == 6) {
    if (g < 3) {
      const int jglob = cgp * 3 + g;
      pst8(mt + (((size_t)jglob * 18 + 16 + (hp >> 3)) * 64 + ((hp >> 1) & 3) * 16 + n16) * 8 + (hp & 1) * 4, mrow4(32, g));
    }
  } else if (cgp == 0) {
    const int hrow = (hp >> 3) * 16 + n16;
    float o[8];
#pragma unroll
    for (int e8 = 0; e8 < 8; ++e8) {
      const int c = 16 * (e8 >> 2) + 4 * g + (e8 & 3), hh = c >= 18 ? 1 : 0;
      o[e8] = (2 * hp + hh == hrow) ? kvs[hh][324 + c - 18 * hh] : 0.f;
    }
    pst16(zt + ((size_t)hp * 64 + lane) * 8, u32x4{pk2(o[0], o[1]), pk2(o[2], o[3]), pk2(o[4], o[5]), pk2(o[6], o[7])});
    if (g == 0) {
      const bool mine = (2 * hp + 1 == hrow);
      float z4[4];
#pragma unroll
      for (int i = 0; i < 4; ++i) z4[i] = mine ? kvs[1][324 + 14 + i] : 0.f;
      pst8(zt + (((size_t)16 + (hp >> 3)) * 64 + ((hp >> 1) & 3) * 16 + n16) * 8 + (hp & 1) * 4, u32x2{pk2(z4[0], z4[1]), pk2(z4[2], z4[3])});
    }
  }
  TL_END();
}

__global__ __launch_bounds__(576) void k_b(const u16* __restrict__ pq, const u16* __restrict__ mt, const u16* __restrict__ zt, const u16* __restrict__ xr,
                                           u16* __restrict__ x1, float* __restrict__ st TL_PARAM DBG_PARAM) {
  TL_BEGIN();
  __shared__ __attribute__((aligned(16))) char sq[2 * 18432];
  __shared__ float rden[32][33];
  __shared__ float sred[9][32][2];
  const int tid = threadIdx.x, lane = tid & 63;
  const int w = __builtin_amdgcn_readfirstlane(tid >> 6);
  const int xcd = blockIdx.x & 7, idx = blockIdx.x >> 3;
  const int tt = xcd * 8 + (idx >> 2), nt = idx & 3;
  const int g = lane >> 4, t16 = lane & 15;
  RT_BEGIN(); STAMP(0);
  u32x4 pf[4];
#pragma unroll
  for (int i = 0; i < 4; ++i) pf[i] = *(const u32x4*)(pq + ((size_t)tt * 36 + w * 4 + i) * 512 + lane * 8);
  u32x4 zf[9];
  if (w < 4) {
#pragma unroll
    for (int s = 0; s < 9; ++s) zf[s] = *(const u32x4*)(zt + ((size_t)(s < 8 ? (w & 1) * 8 + s : 16 + (w & 1)) * 64 + lane) * 8);
  }
  __builtin_amdgcn_sched_barrier(0);
  u32x2 xres[2];
  const int cb = nt * 9 + w;
#pragma unroll
  for (int m = 0; m < 2; ++m) xres[m] = *(const u32x2*)(xr + (((size_t)(tt * 2 + m) * 18 + (cb >> 1)) * 64 + lane) * 8 + (cb & 1) * 4);
  u32x4 mf[18];
  const u16* mb = mt + ((size_t)(nt * 9 + w) * 18 * 64 + lane) * 8;
#pragma unroll
  for (int s = 0; s < 6; ++s) mf[s] = *(const u32x4*)(mb + (size_t)s * 512);
  __builtin_amdgcn_sched_barrier(0);
#pragma unroll
  for (int i = 0; i < 4; ++i) *(u32x4*)(sq + (w * 4 + i) * 1024 + lane * 16) = pf[i];
  STAMP(1);
  __syncthreads();
  if (w >= 4) {
#pragma unroll
    for (int s = 6; s < 18; ++s) mf[s] = *(const u32x4*)(mb + (size_t)s * 512);
  }
  __builtin_amdgcn_sched_barrier(0);
  if (w < 4) {
    f32x4 d = {0, 0, 0, 0};
#pragma unroll
    for (int s = 0; s < 9; ++s) {
      const u32x4 b = *(const u32x4*)(sq + ((w >> 1) * 18 + (s < 8 ? (w & 1) * 8 + s : 16 + (w & 1))) * 1024 + lane * 16);
      d = MFMA16(__builtin_bit_cast(bf16x8, zf[s]), __builtin_bit_cast(bf16x8, b), d);
    }
#pragma unroll
    for (int i = 0; i < 4; ++i) rden[(w >> 1) * 16 + t16][(w & 1) * 16 + 4 * g + i] = __builtin_amdgcn_rcpf(d[i] + 1e-6f);
#pragma unroll
    for (int s = 6; s < 18; ++s) mf[s] = *(const u32x4*)(mb + (size_t)s * 512);
  }
  __syncthreads();
  STAMP(2);
#pragma unroll
  for (int i = 0; i < 4; ++i) {
    const int f = w * 4 + i, T = f / 18, s = f % 18;
    const float* rd = rden[T * 16 + t16];
    int ha, hb2, hc, hd2;
    if (s < 16) { ha = 2 * s; hb2 = ha; hc = 2 * s + (g > 0 ? 1 : 0); hd2 = 2 * s + 1; }
    else { const int qq = 4 * (s - 16) + g; ha = 4 * qq + 1; hb2 = ha; hc = 4 * qq + 3; hd2 = hc; }
    const float r0 = rd[ha], r1 = rd[hb2], r2 = rd[hc], r3 = rd[hd2];
    const u32x4 v = pf[i];
    *(u32x4*)(sq + f * 1024 + lane * 16) = u32x4{pk2(lo16(v[0]) * r0, hi16(v[0]) * r0), pk2(lo16(v[1]) * r1, hi16(v[1]) * r1), pk2(lo16(v[2]) * r2, hi16(v[2]) * r2), pk2(lo16(v[3]) * r3, hi16(v[3]) * r3)};
  }
  __syncthreads();
  STAMP(3);
  f32x4 acc[2] = {{0, 0, 0, 0}, {0, 0, 0, 0}};
#pragma unroll
  for (int s = 0; s < 18; ++s) {
    const u32x4 b0 = *(const u32x4*)(sq + s * 1024 + lane * 16), b1 = *(const u32x4*)(sq + (18 + s) * 1024 + lane * 16);
    acc[0] = MFMA16(__builtin_bit_cast(bf16x8, mf[s]), __builtin_bit_cast(bf16x8, b0), acc[0]);
    acc[1] = MFMA16(__builtin_bit_cast(bf16x8, mf[s]), __builtin_bit_cast(bf16x8, b1), acc[1]);
  }
  STAMP(4);
#pragma unroll
  for (int m = 0; m < 2; ++m) {
    const unsigned o0 = pk2(acc[m][0] + lo16(xres[m][0]), acc[m][1] + hi16(xres[m][0])), o1 = pk2(acc[m][2] + lo16(xres[m][1]), acc[m][3] + hi16(xres[m][1]));
    pst8(x1 + (((size_t)(tt * 2 + m) * 18 + (cb >> 1)) * 64 + lane) * 8 + (cb & 1) * 4, u32x2{o0, o1});
    const float a0 = lo16(o0), a1 = hi16(o0), a2 = lo16(o1), a3 = hi16(o1);
    float sm = (a0 + a1) + (a2 + a3), sq2 = (a0 * a0 + a1 * a1) + (a2 * a2 + a3 * a3);
    sm += __shfl_xor(sm, 16); sq2 += __shfl_xor(sq2, 16);
    sm += __shfl_xor(sm, 32); sq2 += __shfl_xor(sq2, 32);
    if (g == 0) { sred[w][m * 16 + t16][0] = sm; sred[w][m * 16 + t16][1] = sq2; }
  }
  __syncthreads();
  if (tid < 64) {
    const int t = tid >> 1, c = tid & 1;
    float a = 0.f;
#pragma unroll
    for (int i = 0; i < 9; ++i) a += sred[i][t][c];
    pst4(st + ((size_t)(tt * 32 + t) * 4 + nt) * 2 + c, a);
  }
  STAMP(5);
  RT_END();
  TL_END();
}

#define FLUSHL2()
extern "C" void kernel_launch(void* const* d_in, const int* in_sizes, int n_in, void* d_out, int out_size, void* d_ws, size_t ws_size, hipStream_t stream) {
  const u16* x = (const u16*)d_in[0]; const u16* wqkv = (const u16*)d_in[1]; const u16* wo = (const u16*)d_in[2];
  const u16* wg = (const u16*)d_in[4]; const u16* wu = (const u16*)d_in[5]; const u16* wd = (const u16*)d_in[6];
  char* ws = (char*)d_ws; size_t off = 0; auto al = [&](size_t b) { void* p = ws + off; off += (b + 1023) / 1024 * 1024; return p; };
  u16* xr = (u16*)al((size_t)S_ * D_ * 2); u16* xn = (u16*)al((size_t)S_ * D_ * 2); u16* wt = (u16*)al((size_t)2016 * 1024);
  u16* pq = (u16*)al((size_t)S_ * D_ * 2); float* kvp = (float*)al((size_t)16 * 32 * 344 * 4); u16* mt = (u16*)al((size_t)36 * 18 * 1024); u16* zt = (u16*)al(18 * 1024);
  u16* x1 = (u16*)al((size_t)S_ * D_ * 2); float* st = (float*)al((size_t)S_ * 8 * 4); u16* h = (u16*)al((size_t)S_ * F_ * 2); u16* part = (u16*)al((size_t)4 * S_ * D_ * 2);
  unsigned long long* dbg = nullptr;
  (void)hipFuncSetAttribute((const void*)k_a, hipFuncAttributeMaxDynamicSharedMemorySize, A_LDS);
  (void)hipFuncSetAttribute((const void*)k_c, hipFuncAttributeMaxDynamicSharedMemorySize, C_LDS);
  (void)hipFuncSetAttribute((const void*)k_d, hipFuncAttributeMaxDynamicSharedMemorySize, D_LDS);
  for (int l = 0; l < 2; ++l) {
    if (l == 0) k_r<0><<<256 + 224, 576, 0, stream>>>(x, xr, xn, nullptr, wqkv, wt TL_ARG(l * 6 + 0));
    else k_r<1><<<256, 576, 0, stream>>>(part, xr, xn, nullptr, nullptr, nullptr TL_ARG(l * 6 + 0));
    k_a<<<256, 512, A_LDS, stream>>>(xn, wt, pq, kvp TL_ARG(l * 6 + 1) DBG_ARG(2));
    k_a2<<<192, 512, 0, stream>>>(kvp, wo, mt, zt TL_ARG(l * 6 + 2));
    k_b<<<256, 576, 0, stream>>>(pq, mt, zt, xr, x1, st TL_ARG(l * 6 + 3) DBG_ARG(3));
    k_c<<<256, 512, C_LDS, stream>>>(x1, st, wg, wu, h, dbg TL_ARG(l * 6 + 4));
    k_d<<<256, 512, D_LDS, stream>>>(h, wd, x1, part, dbg ? dbg + 256 * 32 : dbg TL_ARG(l * 6 + 5));
  }
  k_r<2><<<256, 576, 0, stream>>>(part, nullptr, nullptr, (u16*)d_out, nullptr, nullptr TL_ARG(12));
}
```

```cpp
#include <hip/hip_runtime.h>
#include <hip/hip_bf16.h>
#include <stdint.h>

#define LAS __attribute__((address_space(3)))
#define GAS __attribute__((address_space(1)))
#define DI __device__ __forceinline__
typedef __attribute__((ext_vector_type(8))) __bf16 bf16x8;
typedef __attribute__((ext_vector_type(4))) __bf16 bf16x4;
typedef __attribute__((ext_vector_type(2))) __bf16 bf16x2;
typedef __attribute__((ext_vector_type(4))) short s16x4;
typedef __attribute__((ext_vector_type(8))) short s16x8;
typedef __attribute__((ext_vector_type(4))) float f32x4;
typedef __attribute__((ext_vector_type(2))) float f32x2;
typedef __attribute__((ext_vector_type(4))) unsigned u32x4;
typedef __attribute__((ext_vector_type(2))) unsigned u32x2;
typedef unsigned short u16;

constexpr int S_ = 2048, D_ = 576, F_ = 4608;
#define MFMA16(a, b, c) __builtin_amdgcn_mfma_f32_16x16x32_bf16((a), (b), (c), 0, 0, 0)

DI float bf2f(u16 b) { return __uint_as_float((unsigned)b << 16); }
DI u16 f2bf(float f) { __bf16 b = (__bf16)f; return __builtin_bit_cast(u16, b); }
DI unsigned pk2(float a, float b) { f32x2 v = {a, b}; bf16x2 r = __builtin_convertvector(v, bf16x2); return __builtin_bit_cast(unsigned, r); }
DI float lo16(unsigned u) { return __uint_as_float(u << 16); }
DI float hi16(unsigned u) { return __uint_as_float(u & 0xffff0000u); }
__host__ __device__ __forceinline__ size_t hf(int t, int c, int C) { return (((size_t)(t >> 4) * (C >> 5) + (c >> 5)) * 64 + ((c >> 2) & 3) * 16 + (t & 15)) * 8 + ((c >> 4) & 1) * 4 + (c & 3); }

DI void glds16(unsigned voff, const void* sbase, unsigned lds) {
  asm volatile("s_mov_b32 m0, %2\n\ts_nop 0\n\tglobal_load_lds_dwordx4 %0, %1" :: "v"(voff), "s"(sbase), "s"(lds) : "memory");
}
#define WAITV(n) asm volatile("s_waitcnt vmcnt(%0)" :: "n"(n) : "memory")
#define WAITL0() asm volatile("s_waitcnt lgkmcnt(0)" ::: "memory")
#define BAR() do { asm volatile("" ::: "memory"); __builtin_amdgcn_s_barrier(); asm volatile("" ::: "memory"); } while (0)

DI bf16x8 ld_frag(LAS char* p) { return __builtin_bit_cast(bf16x8, *(LAS u32x4*)p); }
#define STAMP(i) do {} while (0)
#define TL_PARAM
#define TL_ARG(k)
#define TL_BEGIN() do {} while (0)
#define TL_END() do {} while (0)
#define DBG_PARAM
#define DBG_ARG(k)
#define RT_BEGIN() do {} while (0)
#define RT_END() do {} while (0)
#define SB0() __builtin_amdgcn_sched_barrier(0)
#define SGB(m, n) __builtin_amdgcn_sched_group_barrier((m), (n), 0)
DI bf16x8 ld_tr(LAS char* p0, LAS char* p1) {
  s16x4 a = __builtin_amdgcn_ds_read_tr16_b64_v4i16((LAS s16x4*)p0);
  s16x4 b = __builtin_amdgcn_ds_read_tr16_b64_v4i16((LAS s16x4*)p1);
  s16x8 r = __builtin_shufflevector(a, b, 0, 1, 2, 3, 4, 5, 6, 7);
  return __builtin_bit_cast(bf16x8, r);
}
DI float geglu1(float g, float u) {
  const float c1 = -2.0f * 0.7978845608028654f * 1.4426950408889634f;
  float p = __builtin_fmaf(g * g, c1 * 0.044715f, c1);
  float e = __builtin_amdgcn_exp2f(p * g);
  float r = __builtin_amdgcn_rcpf(1.0f + e);
  return g * u * r;
}

constexpr int XSLOT = 18432;
constexpr int C_LDS = 4 * XSLOT + 5 * 16384 + 304 * 16;
#define AMF(acc, a, b) asm volatile("v_mfma_f32_16x16x32_bf16 %0, %1, %2, %0" : "+v"(acc) : "v"(a), "v"(b))
#define AMF0(acc, a, b) asm volatile("v_mfma_f32_16x16x32_bf16 %0, %1, %2, 0" : "=&v"(acc) : "v"(a), "v"(b))
#define WAITLG(n) __builtin_amdgcn_s_waitcnt(0xC07F | ((n) << 8))
#define PIN(x) asm volatile("" : "+v"(x))
#define HST "sc1"
DI void gstore8(unsigned voff, const void* sbase, u32x2 v) { asm volatile("global_store_dwordx2 %0, %1, %2 " HST :: "v"(voff), "v"(v), "s"(sbase) : "memory"); }
DI void pst4(void* p, float v) { *(GAS float*)p = v; }
DI void pst16(void* p, u32x4 v) { *(GAS u32x4*)p = v; }
DI void pst8(void* p, u32x2 v) { *(GAS u32x2*)p = v; }
constexpr int cstep(int k) { return k > 17 ? 0 : 2 + ((k & 1) ? 0 : 1); }
constexpr int csum(int a, int b) { int r = 0; for (int k = a; k <= b; ++k) r += cstep(k); return r; }

__global__ __launch_bounds__(512, 2) void k_c(const u16* __restrict__ x1, const float* __restrict__ st, const u16* __restrict__ Wg,
                                              const u16* __restrict__ Wu, u16* __restrict__ h, unsigned long long* dbg TL_PARAM) {
  extern __shared__ __attribute__((aligned(16))) char smem_[];
  LAS char* smem = (LAS char*)smem_;
  const int tid = threadIdx.x, lane = tid & 63;
  const int w = __builtin_amdgcn_readfirstlane(tid >> 6);
  const int xcd = blockIdx.x & 7, idx = blockIdx.x >> 3;
  const int start = (63 * xcd + 1) >> 1, cnt = ((63 * (xcd + 1) + 1) >> 1) - start;
  if (idx >= cnt) return;
  TL_BEGIN();
  STAMP(0);
  const int L = start + idx, slice = L / 7, range = L % 7;
  const int ntile = range < 2 ? 19 : 18, tile0 = range < 2 ? 19 * range : 38 + 18 * (range - 2);
  const unsigned ldsb = (unsigned)(size_t)smem;
  const int g = lane >> 4, t16 = lane & 15;

  u32x4 s0, s1;
  { const unsigned so = (unsigned)min(tid, ntile * 16 - 1) * 32u; const float* sb = st + (size_t)tile0 * 16 * 8;
    asm volatile("global_load_dwordx4 %0, %1, %2" : "=v"(s0) : "v"(so), "s"(sb) : "memory");
    asm volatile("global_load_dwordx4 %0, %1, %2 offset:16" : "=v"(s1) : "v"(so), "s"(sb) : "memory"); }

  const char* xb = (const char*)x1 + (size_t)tile0 * XSLOT;
  const unsigned vlin = lane * 16;
  const int row32 = 4 * w + g;
  const unsigned wvoff = row32 * (F_ * 2) + ((t16 ^ (2 * (row32 & 7))) * 16);
  const char* wgb = (const char*)Wg + slice * 256;
  const char* wub = (const char*)Wu + slice * 256;
  constexpr int WR0 = 4 * XSLOT;
  auto wslot = [&](int k) -> int { return WR0 + ((k + 2) % 5) * 16384; };
  auto issueW = [&](int k, int mat) {
    glds16(wvoff, (mat ? wub : wgb) + (size_t)k * 32 * (F_ * 2), ldsb + wslot(k) + mat * 8192 + w * 1024);
  };
  auto issueX = [&](int s) {
    const int o = (w & 3) * XSLOT + (s + (w >> 2)) * 1024; glds16(vlin, xb + o, ldsb + o);
  };
  const unsigned vp0 = vlin + w * 1024, vp1 = vp0 + 8192, vp2 = vp0 + 16384;
  const unsigned ldw = ldsb + w * 1024;
  auto load_piece = [&](int T, int pc) {
    const int Tc = min(T, ntile - 1);
    const char* src = xb + Tc * XSLOT; const unsigned dst = ldw + (T & 7) * XSLOT + pc * 8192;
    if (pc < 2 || w < 2) glds16(pc == 0 ? vp0 : pc == 1 ? vp1 : vp2, src, dst);
  };
  constexpr int LA = 5;
#pragma unroll
  for (int s = 0; s < LA; ++s) { if ((s & 1) == 0) issueX(s); issueW(s, 0); issueW(s, 1); if (s < LA - 1) BAR(); }

  bf16x8 wg[18], wu[18];
  f32x4 a1g[4], a1u[4], cg, cu;
  const u32x4 ones_u = {0x3f803f80u, 0x3f803f80u, 0x3f803f80u, 0x3f803f80u};
  bf16x8 ones = __builtin_bit_cast(bf16x8, ones_u);
  PIN(ones);
  const int q = t16 >> 2, p = lane & 3, rrow = 4 * g + q;
  const unsigned tr_off = rrow * 256 + (((2 * w + (p >> 1)) ^ (2 * (rrow & 7))) * 16) + 8 * (p & 1);
  LAS char* xl = smem + lane * 16;
  LAS char* wtr = smem + tr_off;
  bf16x8 xf1[2][4];
  constexpr int STATS = WR0 + 5 * 16384;
  {
    asm volatile("s_waitcnt vmcnt(%2)" : "+v"(s0), "+v"(s1) : "n"(csum(2, LA - 1)) : "memory");
    STAMP(1);
    float sum = __uint_as_float(s0[0]) + __uint_as_float(s0[2]) + __uint_as_float(s1[0]) + __uint_as_float(s1[2]);
    float sq = __uint_as_float(s0[1]) + __uint_as_float(s0[3]) + __uint_as_float(s1[1]) + __uint_as_float(s1[3]);
    float mean = sum * (1.0f / D_), var = sq * (1.0f / D_) - mean * mean;
    const float irs2 = fmaxf(var, 0.f) + 1e-5f, rs = __builtin_amdgcn_rsqf(irs2);
    const float c1 = -2.0f * 0.7978845608028654f * 1.4426950408889634f;
    if (tid < 304) *(LAS f32x4*)(smem + STATS + tid * 16) = f32x4{-mean, c1 * rs, c1 * 0.044715f * rs * rs * rs, irs2};
    WAITL0(); BAR();
    wg[0] = ld_tr(wtr + wslot(0), wtr + wslot(0) + 4096);
    wu[0] = ld_tr(wtr + wslot(0) + 8192, wtr + wslot(0) + 8192 + 4096);
#pragma unroll
    for (int T = 0; T < 4; ++T) xf1[0][T] = ld_frag(xl + T * XSLOT);
  }
#define P1MF(acc, W_, X_) do { if (s == 0) AMF0(acc, W_, X_); else AMF(acc, W_, X_); } while (0)
  const int half = w >> 2;
  auto vop = [&](int s, int n) {
    const int k = s + LA;
    if (k < 18) {
      if (n == 0) { if ((k & 1) == 0) issueX(k); }
      else if (n == 1) issueW(k, 0);
      else issueW(k, 1);
    } else {
      if (s >= 14) load_piece(s - 10, n);
    }
  };
#pragma unroll
  for (int s = 0; s < 18; ++s) {
    SB0();
    P1MF(a1g[0], wg[s], xf1[s & 1][0]);
    if (s < 17) wg[s + 1] = ld_tr(wtr + wslot(s + 1), wtr + wslot(s + 1) + 4096);
    if (half == 0) vop(s, 0);
    SB0();
    P1MF(a1u[0], wu[s], xf1[s & 1][0]);
    if (s < 17) wu[s + 1] = ld_tr(wtr + wslot(s + 1) + 8192, wtr + wslot(s + 1) + 8192 + 4096);
    SB0();
    P1MF(a1g[1], wg[s], xf1[s & 1][1]);
    if (s < 17) xf1[(s + 1) & 1][0] = ld_frag(xl + 0 * XSLOT + (s + 1) * 1024);
    if (half == 1) vop(s, 0);
    SB0();
    P1MF(a1u[1], wu[s], xf1[s & 1][1]);
    if (s < 17) xf1[(s + 1) & 1][1] = ld_frag(xl + 1 * XSLOT + (s + 1) * 1024);
    if (half == 0) vop(s, 1);
    SB0();
    P1MF(a1g[2], wg[s], xf1[s & 1][2]);
    if (s < 17) xf1[(s + 1) & 1][2] = ld_frag(xl + 2 * XSLOT + (s + 1) * 1024);
    SB0();
    P1MF(a1u[2], wu[s], xf1[s & 1][2]);
    if (s < 17) xf1[(s + 1) & 1][3] = ld_frag(xl + 3 * XSLOT + (s + 1) * 1024);
    if (half == 1) vop(s, 1);
    SB0();
    P1MF(a1g[3], wg[s], xf1[s & 1][3]);
    if (half == 0) vop(s, 2);
    SB0();
    P1MF(a1u[3], wu[s], xf1[s & 1][3]);
    SB0();
    P1MF(cg, wg[s], ones);
    if (half == 1) vop(s, 2);
    SB0();
    P1MF(cu, wu[s], ones);
    SB0();
    if (s < 17) {
      if (s + 2 < 18) {
        switch (s) {
#define WCASE(S) case S: WAITV(csum(S + 3, (S + LA < 17 ? S + LA : 17)) + (S >= 14 ? (S - 13) * 2 : 0)); break;
          WCASE(0) WCASE(1) WCASE(2) WCASE(3) WCASE(4) WCASE(5) WCASE(6) WCASE(7) WCASE(8) WCASE(9) WCASE(10) WCASE(11) WCASE(12) WCASE(13) WCASE(14) WCASE(15)
#undef WCASE
        }
      }
      WAITL0(); BAR();
    }
  }
  STAMP(2);
  WAITV(6); WAITL0(); BAR();

  const char* hb = (const char*)h + ((size_t)tile0 * (F_ / 32) + slice * 4 + (w >> 1)) * 1024;
  const unsigned hvo = lane * 16 + (w & 1) * 8;
  constexpr size_t HT = (size_t)(F_ / 32) * 1024;
  auto epi = [&](const f32x4& ag, const f32x4& au, int T) -> u32x2 {
    f32x4 sv = *(LAS f32x4*)(smem + STATS + (T * 16 + t16) * 16);
    float hv[4];
#pragma unroll
    for (int i = 0; i < 4; ++i) {
      float tg = __builtin_fmaf(sv[0], cg[i], ag[i]), tu = __builtin_fmaf(sv[0], cu[i], au[i]);
      float y = __builtin_fmaf(tg * tg, sv[2], sv[1]) * tg;
      float e = __builtin_amdgcn_exp2f(y);
      float r = __builtin_amdgcn_rcpf(__builtin_fmaf(e, sv[3], sv[3]));
      hv[i] = tg * tu * r;
    }
    return u32x2{pk2(hv[0], hv[1]), pk2(hv[2], hv[3])};
  };
  constexpr int PD = 4;
  bf16x8 xr[6];
#pragma unroll
  for (int s = 0; s < PD; ++s) xr[s] = ld_frag(xl + 4 * XSLOT + s * 1024);
  asm volatile("s_nop 7\n\ts_nop 1" : "+v"(a1g[0]), "+v"(a1g[1]), "+v"(a1g[2]), "+v"(a1g[3]), "+v"(a1u[0]), "+v"(a1u[1]), "+v"(a1u[2]), "+v"(a1u[3]), "+v"(cg), "+v"(cu));
#pragma unroll
  for (int T = 0; T < 3; ++T) { u32x2 o = epi(a1g[T], a1u[T], T); gstore8(hvo, hb + (size_t)T * HT, o); }
  STAMP(3);
  const int ph = half;
  auto tile = [&](int T, f32x4& ag, f32x4& au, const f32x4& pg, const f32x4& pu) {
    LAS char* xt = xl + (T & 7) * XSLOT;
    LAS char* xn = xl + ((T + 1) & 7) * XSLOT;
    f32x4 sv = *(LAS f32x4*)(smem + STATS + ((T - 1) * 16 + t16) * 16);
    float tg[2], tu[2], y[2], e[2], m[2], r[2];
    unsigned o[2] = {0, 0};
    const char* nsrc = xb + min(T + 4, ntile - 1) * XSLOT; const unsigned ndst = ldw + ((T + 4) & 7) * XSLOT;
    auto eslot = [&](int q) {
      if (q < 0 || q >= 24) return;
      const int pr = q / 12, op = q % 12, ia = 2 * pr, ib = 2 * pr + 1;
#define SEP(x)
      switch (op) {
        case 0: tg[0] = __builtin_fmaf(sv[0], cg[ia], pg[ia]); SEP(tg[0]); tg[1] = __builtin_fmaf(sv[0], cg[ib], pg[ib]); PIN(tg[0]); PIN(tg[1]); break;
        case 1: tu[0] = __builtin_fmaf(sv[0], cu[ia], pu[ia]); SEP(tu[0]); tu[1] = __builtin_fmaf(sv[0], cu[ib], pu[ib]); PIN(tu[0]); PIN(tu[1]); break;
        case 2: y[0] = tg[0] * tg[0]; SEP(y[0]); y[1] = tg[1] * tg[1]; PIN(y[0]); PIN(y[1]); break;
        case 3: y[0] = __builtin_fmaf(y[0], sv[2], sv[1]); SEP(y[0]); y[1] = __builtin_fmaf(y[1], sv[2], sv[1]); PIN(y[0]); PIN(y[1]); break;
        case 4: y[0] = y[0] * tg[0]; SEP(y[0]); y[1] = y[1] * tg[1]; PIN(y[0]); PIN(y[1]); break;
        case 5: e[0] = __builtin_amdgcn_exp2f(y[0]); m[0] = tg[0] * tu[0]; PIN(e[0]); PIN(m[0]); break;
        case 6: e[1] = __builtin_amdgcn_exp2f(y[1]); m[1] = tg[1] * tu[1]; PIN(e[1]); PIN(m[1]); break;
        case 7: e[0] = __builtin_fmaf(e[0], sv[3], sv[3]); SEP(e[0]); e[1] = __builtin_fmaf(e[1], sv[3], sv[3]); PIN(e[0]); PIN(e[1]); break;
        case 8: r[0] = __builtin_amdgcn_rcpf(e[0]); PIN(r[0]); break;
        case 9: r[1] = __builtin_amdgcn_rcpf(e[1]); PIN(r[1]); break;
        case 10: m[0] = m[0] * r[0]; SEP(m[0]); m[1] = m[1] * r[1]; PIN(m[0]); PIN(m[1]); break;
        default: o[pr] = pk2(m[0], m[1]); PIN(o[pr]); break;
      }
    };
    SB0();
#pragma unroll
    for (int s = 0; s < 18; ++s) {
      if (s == 8) {
        WAITV(4);
        BAR();
        SB0();
      }
      if (s == 0) AMF0(ag, wg[0], xr[0]); else AMF(ag, wg[s], xr[s % 6]);
      SB0();
      if (s + PD < 18) xr[(s + PD) % 6] = ld_frag(xt + (s + PD) * 1024); else xr[(s + PD) % 6] = ld_frag(xn + (s + PD - 18) * 1024);
      eslot(2 * s - 4);
      SB0();
      if (s == 0) AMF0(au, wu[0], xr[0]); else AMF(au, wu[s], xr[s % 6]);
      SB0();
      eslot(2 * s - 3);
      if (s >= 9 && s <= 14) { const int pc = (s - 9) >> 1; if (((s - 9) & 1) == ph && (pc < 2 || w < 2)) glds16(pc == 0 ? vp0 : pc == 1 ? vp1 : vp2, nsrc, ndst + pc * 8192); }
      if (s == 15) gstore8(hvo, hb + (size_t)(T - 1) * HT, u32x2{o[0], o[1]});
      SB0();
    }
  };
  f32x4 Ag, Au, Bg, Bu;
  int T = 4;
  if (ntile == 19) { tile(4, Bg, Bu, a1g[3], a1u[3]); asm volatile("s_nop 7\n\ts_nop 1" : "+v"(Bg), "+v"(Bu)); T = 5; } else { Bg = a1g[3]; Bu = a1u[3]; }
  for (; T < ntile; T += 2) {
    tile(T, Ag, Au, Bg, Bu);
    tile(T + 1, Bg, Bu, Ag, Au);
  }
  STAMP(4);
  asm volatile("s_nop 7\n\ts_nop 1" : "+v"(Bg), "+v"(Bu));
  { u32x2 o = epi(Bg, Bu, ntile - 1); gstore8(hvo, hb + (size_t)(ntile - 1) * HT, o); }
  STAMP(5);
  TL_END();
}

constexpr int D_STAGE = 2 * 2 * 12288;
constexpr int D_LDS = 3 * D_STAGE;
__global__ __launch_bounds__(512, 2) void k_d(const u16* __restrict__ h, const u16* __restrict__ Wd, const u16* __restrict__ x1, u16* __restrict__ part, unsigned long long* dbg TL_PARAM) {
  TL_BEGIN();
  extern __shared__ __attribute__((aligned(16))) char smem_[];
  LAS char* smem = (LAS char*)smem_;
  const int tid = threadIdx.x, lane = tid & 63;
  const int w = __builtin_amdgcn_readfirstlane(tid >> 6);
  const int xcd = blockIdx.x & 7, idx = blockIdx.x >> 3;
  const int ks = xcd & 3, tt = (xcd >> 2) * 8 + (idx >> 2), nt = idx & 3;
  const int mq = w & 3, kh = w >> 2;
  const unsigned ldsb = (unsigned)(size_t)smem;
  const int g = lane >> 4, t16 = lane & 15;
  STAMP(0);
  unsigned wv[3];
#pragma unroll
  for (int i = 0; i < 3; ++i) { int qq = 64 * i + lane, k = qq / 24, pp = qq % 24; int c = (pp & ~7) | ((pp & 7) ^ (2 * ((k >> 1) & 3))); wv[i] = k * (D_ * 2) + c * 16; }
  const int krow0 = ks * 1152 + kh * 576;
  const char* wb = (const char*)Wd + (size_t)(krow0 + 8 * mq) * (D_ * 2) + nt * 256;
  const int inner = nt * 32;
  const int rot = 0;
  auto iof = [&](int it) { int k = it + rot; return k >= 9 ? k - 9 : k; };
  auto stageW1 = [&](int it, int n) {
    const int u = n / 3, i = n % 3;
    const unsigned dst = ldsb + (it % 3) * D_STAGE + kh * 24576 + mq * 3072;
    glds16(wv[i], wb + (size_t)(iof(it) * 2 + u) * 32 * (D_ * 2), dst + u * 12288 + i * 1024);
  };
  const char* hb = (const char*)h + ((size_t)(tt * 8 + 2 * mq) * (F_ / 32) + krow0 / 32) * 1024;
  const unsigned hv = lane * 16;
  u32x4 hr[3][4];
  auto loadH1 = [&](int it, int n, u32x4& r) {
    const int u = n >> 1, m = n & 1;
    asm volatile("global_load_dwordx4 %0, %1, %2" : "=v"(r) : "v"(hv), "s"(hb + (size_t)m * (F_ / 32) * 1024 + (iof(it) * 2 + u) * 1024) : "memory");
  };
  stageW1(0, 0); stageW1(0, 1); stageW1(0, 2); loadH1(0, 0, hr[0][0]); loadH1(0, 1, hr[0][1]);
  BAR();
  stageW1(0, 3); stageW1(0, 4); stageW1(0, 5); loadH1(0, 2, hr[0][2]); loadH1(0, 3, hr[0][3]);
  BAR();
#pragma unroll
  for (int n = 0; n < 6; ++n) stageW1(1, n);
#pragma unroll
  for (int n = 0; n < 4; ++n) loadH1(1, n, hr[1][n]);
  stageW1(2, 0);
  f32x4 acc[2][9];
  const int q = t16 >> 2, p = lane & 3, rr = 4 * g + q;
  const int sw = 2 * ((rr >> 1) & 3);
  LAS char* wl = smem + kh * 24576 + rr * 384 + 8 * (p & 1);
  unsigned poff[9];
#pragma unroll
  for (int j = 0; j < 9; ++j) { const int c = inner / 16 + 2 * j + (p >> 1); poff[j] = ((c & ~7) | ((c & 7) ^ sw)) * 16; }
  constexpr int WPD = 3;
  bf16x8 wf[6];
  auto rdW = [&](int it, int i) -> bf16x8 { const int u = i / 9, j = i % 9; LAS char* a = wl + (it % 3) * D_STAGE + u * 12288 + poff[j]; return ld_tr(a, a + 16 * 384); };
  asm volatile("s_waitcnt vmcnt(16)" : "+v"(hr[0][0]), "+v"(hr[0][1]) :: "memory");
  STAMP(1);
  BAR();
#pragma unroll
  for (int i = 0; i < WPD; ++i) wf[i] = rdW(0, i);
#pragma unroll
  for (int it = 0; it < 9; ++it) {
    u32x4(&r)[4] = hr[it % 3];
    SB0();
#pragma unroll
    for (int i = 0; i < 18; ++i) {
      const int u = i / 9, j = i % 9;
      if (it == 0 && i == 9 - WPD) {
        asm volatile("s_waitcnt vmcnt(14)" : "+v"(hr[0][2]), "+v"(hr[0][3]) :: "memory");
        BAR();
        SB0();
      }
      if (i == 18 - WPD && it < 8) {
        u32x4(&rn)[4] = hr[(it + 1) % 3];
        if (it < 7) asm volatile("s_waitcnt vmcnt(9) lgkmcnt(0)" : "+v"(rn[0]), "+v"(rn[1]), "+v"(rn[2]), "+v"(rn[3]) :: "memory");
        else asm volatile("s_waitcnt vmcnt(0) lgkmcnt(0)" : "+v"(rn[0]), "+v"(rn[1]), "+v"(rn[2]), "+v"(rn[3]) :: "memory");
        BAR();
        SB0();
      }
      if (i + WPD < 18) wf[(i + WPD) % 6] = rdW(it, i + WPD);
      else if (it < 8) wf[(i + WPD) % 6] = rdW(it + 1, i + WPD - 18);
      if (it == 0 && u == 0) { AMF0(acc[0][j], wf[i % 6], r[u * 2 + 0]); AMF0(acc[1][j], wf[i % 6], r[u * 2 + 1]); }
      else { AMF(acc[0][j], wf[i % 6], r[u * 2 + 0]); AMF(acc[1][j], wf[i % 6], r[u * 2 + 1]); }
      if (it + 2 < 9) {
        if (i <= 8 && (i & 1) == 0) { if (i > 0 || true) { const int n = 1 + i / 2; if (n <= 5) stageW1(it + 2, n); } }
        if (i == 10 || i == 12 || i == 14) loadH1(it + 2, (i - 10) / 2, hr[(it + 2) % 3][(i - 10) / 2]);
        if (i == 16) loadH1(it + 2, 3, hr[(it + 2) % 3][3]);
      }
      if (i == 17 && it + 3 < 9) stageW1(it + 3, 0);
      SB0();
    }
  }
  STAMP(2);
  asm volatile("s_nop 7\n\ts_nop 1" ::: "memory");
  const int Tg = tt * 8 + 2 * mq + kh;
  const u16* xrow = x1 + ((size_t)Tg * 18 * 64 + lane) * 8;
  u32x2 xres[9];
#pragma unroll
  for (int j = 0; j < 9; ++j) xres[j] = u32x2{0, 0};
  if (ks == 0) {
#pragma unroll
    for (int j = 0; j < 9; ++j) { const int cb = nt * 9 + j; xres[j] = *(const GAS u32x2*)(xrow + (cb >> 1) * 512 + (cb & 1) * 4); }
  }
  LAS f32x4* red = (LAS f32x4*)smem;
  u32x2 o[9];
  auto fin = [&](f32x4 (&keep)[9], f32x4 (&send)[9]) {
#pragma unroll
    for (int j = 0; j < 9; ++j) red[(w * 9 + j) * 64 + lane] = send[j];
    WAITL0(); BAR();
#pragma unroll
    for (int j = 0; j < 9; ++j) {
      f32x4 v = keep[j] + red[((w ^ 4) * 9 + j) * 64 + lane];
      v[0] += lo16(xres[j][0]); v[1] += hi16(xres[j][0]); v[2] += lo16(xres[j][1]); v[3] += hi16(xres[j][1]);
      o[j] = u32x2{pk2(v[0], v[1]), pk2(v[2], v[3])};
    }
  };
  if (kh == 0) fin(acc[0], acc[1]); else fin(acc[1], acc[0]);
  u16* prow = part + (size_t)ks * S_ * D_ + ((size_t)Tg * 18 * 64 + lane) * 8;
  const int cb0 = nt * 9;
  if ((nt & 1) == 0) {
#pragma unroll
    for (int j = 0; j < 8; j += 2) pst16(prow + ((cb0 + j) >> 1) * 512, u32x4{o[j][0], o[j][1], o[j + 1][0], o[j + 1][1]});
    pst8(prow + ((cb0 + 8) >> 1) * 512, o[8]);
  } else {
    pst8(prow + (cb0 >> 1) * 512 + 4, o[0]);
#pragma unroll
    for (int j = 1; j < 9; j += 2) pst16(prow + ((cb0 + j) >> 1) * 512, u32x4{o[j][0], o[j][1], o[j + 1][0], o[j + 1][1]});
  }
  STAMP(3);
  TL_END();
}

template <int MODE>
__global__ __launch_bounds__(576) void k_r(const u16* __restrict__ src, u16* __restrict__ xr, u16* __restrict__ xn, u16* __restrict__ outp,
                                           const u16* __restrict__ wqkv, u16* __restrict__ wt TL_PARAM) {
  TL_BEGIN();
  const int tid = threadIdx.x, lane = tid & 63, w = tid >> 6;
  if (MODE == 0 && blockIdx.x >= 256) {
    const int f = (blockIdx.x - 256) * 9 + w;
    if (f < 16 * 7 * 18) {
      const int s = f % 18, j = (f / 18) % 7, hp = f / 126;
      const int n16 = lane & 15, g = lane >> 4;
      int col; bool valid = true;
      if (j < 6) col = (j >> 1) * 576 + 36 * hp + (j & 1) * 16 + n16;
      else { const int sec = n16 >> 2; valid = sec < 3; col = (valid ? sec : 0) * 576 + 36 * hp + 32 + (n16 & 3); }
      unsigned v[8];
#pragma unroll
      for (int e = 0; e < 8; ++e) { const int k = 32 * s + 16 * (e >> 2) + 4 * g + (e & 3); v[e] = valid ? wqkv[(size_t)k * 1728 + col] : 0; }
      u32x4 o = {v[0] | (v[1] << 16), v[2] | (v[3] << 16), v[4] | (v[5] << 16), v[6] | (v[7] << 16)};
      pst16(wt + ((size_t)f * 64 + lane) * 8, o);
    }
    TL_END();
    return;
  }
  __shared__ float red[9][8][2];
  const int t8 = tid & 7, cg = tid >> 3, c32 = cg >> 2, g = cg & 3;
  const int t = blockIdx.x * 8 + t8;
  const size_t ffo = (((size_t)(t >> 4) * 18 + c32) * 64 + g * 16 + (t & 15)) * 8;
  float v[8];
  if (MODE == 0) {
    const u32x2 a = *(const u32x2*)(src + (size_t)t * D_ + 32 * c32 + 4 * g), b = *(const u32x2*)(src + (size_t)t * D_ + 32 * c32 + 16 + 4 * g);
    v[0] = lo16(a[0]); v[1] = hi16(a[0]); v[2] = lo16(a[1]); v[3] = hi16(a[1]); v[4] = lo16(b[0]); v[5] = hi16(b[0]); v[6] = lo16(b[1]); v[7] = hi16(b[1]);
  } else {
    u32x4 p[4];
#pragma unroll
    for (int i = 0; i < 4; ++i) p[i] = *(const u32x4*)(src + (size_t)i * S_ * D_ + ffo);
#pragma unroll
    for (int e = 0; e < 4; ++e) {
      v[2 * e] = (lo16(p[0][e]) + lo16(p[1][e])) + (lo16(p[2][e]) + lo16(p[3][e]));
      v[2 * e + 1] = (hi16(p[0][e]) + hi16(p[1][e])) + (hi16(p[2][e]) + hi16(p[3][e]));
    }
  }
  u32x4 xb = {pk2(v[0], v[1]), pk2(v[2], v[3]), pk2(v[4], v[5]), pk2(v[6], v[7])};
  if (MODE == 2) {
    *(u32x2*)(outp + (size_t)t * D_ + 32 * c32 + 4 * g) = u32x2{xb[0], xb[1]};
    *(u32x2*)(outp + (size_t)t * D_ + 32 * c32 + 16 + 4 * g) = u32x2{xb[2], xb[3]};
    TL_END();
    return;
  }
  pst16(xr + ffo, xb);
#pragma unroll
  for (int e = 0; e < 4; ++e) { v[2 * e] = lo16(xb[e]); v[2 * e + 1] = hi16(xb[e]); }
  float m = ((v[0] + v[1]) + (v[2] + v[3]) + (v[4] + v[5]) + (v[6] + v[7])) * 0.125f, M2 = 0.f;
#pragma unroll
  for (int e = 0; e < 8; ++e) { const float d = v[e] - m; M2 = __builtin_fmaf(d, d, M2); }
  float n = 8.f;
#pragma unroll
  for (int o = 8; o < 64; o <<= 1) {
    const float m2 = __shfl_xor(m, o), q2 = __shfl_xor(M2, o), d = m - m2;
    M2 = M2 + q2 + d * d * (n * 0.5f); m = 0.5f * (m + m2); n *= 2.f;
  }
  if (lane < 8) { red[w][lane][0] = m; red[w][lane][1] = M2; }
  __syncthreads();
  float mt = 0.f;
#pragma unroll
  for (int i = 0; i < 9; ++i) mt += red[i][t8][0];
  mt *= (1.0f / 9.0f);
  float Mt = 0.f;
#pragma unroll
  for (int i = 0; i < 9; ++i) { const float d = red[i][t8][0] - mt; Mt += red[i][t8][1] + d * d * 64.f; }
  const float rs = 1.0f / sqrtf(Mt * (1.0f / D_) + 1e-5f);
  u32x4 o = {pk2((v[0] - mt) * rs, (v[1] - mt) * rs), pk2((v[2] - mt) * rs, (v[3] - mt) * rs), pk2((v[4] - mt) * rs, (v[5] - mt) * rs), pk2((v[6] - mt) * rs, (v[7] - mt) * rs)};
  pst16(xn + ffo, o);
  TL_END();
}

constexpr int A_STAGE = 32768;
constexpr int A_LDS = 4 * A_STAGE;
DI float phi(float t) { return (t > 0.f ? t + 1.0f : __builtin_amdgcn_exp2f(t * 1.4426950408889634f)) + 1e-6f; }
__global__ __launch_bounds__(512, 2) void k_a(const u16* __restrict__ xn, const u16* __restrict__ wt, u16* __restrict__ pq, float* __restrict__ kvp TL_PARAM DBG_PARAM) {
  TL_BEGIN();
  extern __shared__ __attribute__((aligned(16))) char smem_[];
  LAS char* smem = (LAS char*)smem_;
  const int tid = threadIdx.x, lane = tid & 63;
  const int w = __builtin_amdgcn_readfirstlane(tid >> 6);
  const int xcd = blockIdx.x & 7, idx = blockIdx.x >> 3;
  const int tt = (xcd >> 1) * 4 + (idx >> 3), hp = (xcd & 1) * 8 + (idx & 7);
  const unsigned ldsb = (unsigned)(size_t)smem;
  const int g = lane >> 4, t16 = lane & 15;
  const unsigned vlin = lane * 16;
  RT_BEGIN(); STAMP(0);
  const char* xs = (const char*)xn + (size_t)(tt * 8 + w) * 18432;
  const char* wsrc = (const char*)wt + (size_t)(hp * 7 + min(w, 6)) * 18432;
  auto stage1 = [&](int st, int n) {
    const int u = n >> 1;
    const unsigned dst = ldsb + (st & 3) * A_STAGE + w * 1024 + u * 16384;
    if ((n & 1) == 0) glds16(vlin, xs + (2 * st + u) * 1024, dst); else glds16(vlin, wsrc + (2 * st + u) * 1024, dst + 8192);
  };
  stage1(0, 0); stage1(0, 1); BAR(); stage1(0, 2); stage1(0, 3); BAR();
  stage1(1, 0); stage1(1, 1); stage1(1, 2); stage1(1, 3); BAR();
  stage1(2, 0); stage1(2, 1); stage1(2, 2); stage1(2, 3);
  const int tp = w & 3, cgp = w >> 2;
  f32x4 acc[2][4];
#pragma unroll
  for (int m = 0; m < 2; ++m)
#pragma unroll
    for (int j = 0; j < 4; ++j) acc[m][j] = f32x4{0, 0, 0, 0};
  LAS char* xl = smem + lane * 16 + tp * 2048;
  LAS char* wl = smem + lane * 16 + 8192 + cgp * 4096;
#pragma unroll
  for (int st = 0; st < 9; ++st) {
    if (st < 7) WAITV(8); else if (st == 7) WAITV(4); else WAITV(0);
    if (st == 0) STAMP(1);
    WAITL0(); BAR();
#pragma unroll
    for (int u = 0; u < 2; ++u) {
      const int o = (st & 3) * A_STAGE + u * 16384;
      bf16x8 x0 = ld_frag(xl + o), x1 = ld_frag(xl + o + 1024);
      bf16x8 w0 = ld_frag(wl + o), w1 = ld_frag(wl + o + 1024), w2 = ld_frag(wl + o + 2048), w3 = ld_frag(wl + o + (cgp == 0 ? 3072 : 2048));
      SB0();
      if (st + 3 < 9) stage1(st + 3, 2 * u);
      SB0();
      acc[0][0] = MFMA16(w0, x0, acc[0][0]); acc[1][0] = MFMA16(w0, x1, acc[1][0]);
      acc[0][1] = MFMA16(w1, x0, acc[0][1]); acc[1][1] = MFMA16(w1, x1, acc[1][1]);
      SB0();
      if (st + 3 < 9) stage1(st + 3, 2 * u + 1);
      SB0();
      acc[0][2] = MFMA16(w2, x0, acc[0][2]); acc[1][2] = MFMA16(w2, x1, acc[1][2]);
      if (cgp == 0) { acc[0][3] = MFMA16(w3, x0, acc[0][3]); acc[1][3] = MFMA16(w3, x1, acc[1][3]); }
      SB0();
    }
  }
  STAMP(2);
  WAITL0(); BAR();
  constexpr int KVS = 224;
  if (tid < 128) {
    LAS char* r = smem + tid * KVS;
    *(LAS u32x2*)(r + 72) = u32x2{0, 0}; *(LAS u32x2*)(r + 80) = u32x2{0, 0}; *(LAS u32x2*)(r + 88) = u32x2{0, 0};
    *(LAS u32x2*)(r + 168) = u32x2{0x3f80u, 0}; *(LAS u32x2*)(r + 176) = u32x2{0, 0}; *(LAS u32x2*)(r + 184) = u32x2{0, 0};
  }
#pragma unroll
  for (int m = 0; m < 2; ++m) {
    const int tl = (2 * tp + m) * 16 + t16;
    LAS char* row = smem + tl * KVS;
    const size_t Tg = (size_t)(tt * 8 + 2 * tp + m);
    if (cgp == 0) {
      const f32x4 a = acc[m][0], b = acc[m][1];
      pst16(pq + ((Tg * 18 + hp) * 64 + lane) * 8, u32x4{pk2(phi(a[0]), phi(a[1])), pk2(phi(a[2]), phi(a[3])), pk2(phi(b[0]), phi(b[1])), pk2(phi(b[2]), phi(b[3]))});
      *(LAS u32x2*)(row + 8 * g) = u32x2{pk2(phi(acc[m][2][0]), phi(acc[m][2][1])), pk2(phi(acc[m][2][2]), phi(acc[m][2][3]))};
      *(LAS u32x2*)(row + 32 + 8 * g) = u32x2{pk2(phi(acc[m][3][0]), phi(acc[m][3][1])), pk2(phi(acc[m][3][2]), phi(acc[m][3][3]))};
    } else {
      *(LAS u32x2*)(row + 96 + 8 * g) = u32x2{pk2(acc[m][0][0], acc[m][0][1]), pk2(acc[m][0][2], acc[m][0][3])};
      *(LAS u32x2*)(row + 96 + 32 + 8 * g) = u32x2{pk2(acc[m][1][0], acc[m][1][1]), pk2(acc[m][1][2], acc[m][1][3])};
      const f32x4 a = acc[m][2];
      const u32x2 pv = u32x2{pk2(phi(a[0]), phi(a[1])), pk2(phi(a[2]), phi(a[3]))};
      if (g == 0) pst8(pq + ((Tg * 18 + 16 + (hp >> 3)) * 64 + ((hp >> 1) & 3) * 16 + t16) * 8 + (hp & 1) * 4, pv);
      else if (g == 1) *(LAS u32x2*)(row + 64) = pv;
      else if (g == 2) *(LAS u32x2*)(row + 96 + 64) = u32x2{pk2(a[0], a[1]), pk2(a[2], a[3])};
    }
  }
  STAMP(3);
  WAITL0(); BAR();
  const int kt = (0x02211100 >> (4 * w)) & 3, vt = (0x22121010 >> (4 * w)) & 3;
  f32x4 kv = {0, 0, 0, 0};
  const int q = t16 >> 2, p = lane & 3;
#pragma unroll
  for (int s = 0; s < 4; ++s) {
    LAS char* r0 = smem + (32 * s + 4 * g + q) * KVS + 8 * p;
    bf16x8 a = ld_tr(r0 + kt * 32, r0 + 16 * KVS + kt * 32);
    bf16x8 b = ld_tr(r0 + 96 + vt * 32, r0 + 16 * KVS + 96 + vt * 32);
    kv = MFMA16(a, b, kv);
  }
  STAMP(4);
  {
    LAS float* stg = (LAS float*)(smem + 32768);
    const int e = 16 * vt + t16;
#pragma unroll
    for (int i = 0; i < 4; ++i) {
      const int d = 16 * kt + 4 * g + i;
      if (d < 36) {
        const int hd = d >= 18 ? 1 : 0, dd = d - 18 * hd;
        if (e < 36) { const int he = e >= 18 ? 1 : 0; if (he == hd) stg[hd * 344 + dd * 18 + (e - 18 * he)] = kv[i]; }
        else if (e == 36) stg[hd * 344 + 324 + dd] = kv[i];
      }
    }
    if (tid < 4) stg[(tid >> 1) * 344 + 342 + (tid & 1)] = 0.f;
    WAITL0(); BAR();
    if (tid < 172) pst16(kvp + ((size_t)tt * 32 + 2 * hp) * 344 + tid * 4, *(LAS u32x4*)(stg + tid * 4));
  }
  STAMP(5);
  RT_END();
  TL_END();
}

constexpr int KVR = 344;
__global__ __launch_bounds__(512) void k_a2(const float* __restrict__ kvp, const u16* __restrict__ wo, u16* __restrict__ mt, u16* __restrict__ zt TL_PARAM) {
  TL_BEGIN();
  __shared__ __attribute__((aligned(16))) float kvs[2][KVR];
  __shared__ __attribute__((aligned(16))) u16 wos[36][48];
  const int tid = threadIdx.x, lane = tid & 63, w = tid >> 6;
  const int hp = blockIdx.x & 15, cgp = blockIdx.x >> 4;
  u32x4 wv = {0, 0, 0, 0};
  const int wi = tid - 256;
  if (wi >= 0 && wi < 216) wv = *(const u32x4*)(wo + (size_t)(36 * hp + wi / 6) * D_ + cgp * 48 + (wi % 6) * 8);
  if (tid < 172) {
    const f32x4* src = (const f32x4*)(kvp + (size_t)2 * hp * KVR) + tid;
    f32x4 v[16];
#pragma unroll
    for (int p = 0; p < 16; ++p) v[p] = src[(size_t)p * 32 * (KVR / 4)];
    f32x4 a = ((v[0] + v[1]) + (v[2] + v[3])) + ((v[4] + v[5]) + (v[6] + v[7])) + (((v[8] + v[9]) + (v[10] + v[11])) + ((v[12] + v[13]) + (v[14] + v[15])));
#pragma unroll
    for (int i = 0; i < 4; ++i) a[i] = bf2f(f2bf(a[i]));
    *(f32x4*)(&kvs[0][0] + tid * 4) = a;
  }
  if (wi >= 0 && wi < 216) *(u32x4*)&wos[wi / 6][(wi % 6) * 8] = wv;
  __syncthreads();
  const int n16 = lane & 15, g = lane >> 4;
  auto mrow4 = [&](int c0, int ct) -> u32x2 {
    float o[4];
#pragma unroll
    for (int i = 0; i < 4; ++i) {
      const int c = c0 + i, hh = c >= 18 ? 1 : 0, d = c - 18 * hh;
      const f32x2* kr = (const f32x2*)&kvs[hh][d * 18];
      float a = 0.f;
#pragma unroll
      for (int e = 0; e < 9; ++e) {
        const f32x2 kk = kr[e];
        a = __builtin_fmaf(kk[0], bf2f(wos[hh * 18 + 2 * e][ct * 16 + n16]), a);
        a = __builtin_fmaf(kk[1], bf2f(wos[hh * 18 + 2 * e + 1][ct * 16 + n16]), a);
      }
      o[i] = a;
    }
    return u32x2{pk2(o[0], o[1]), pk2(o[2], o[3])};
  };
  if (w < 6) {
    const int ct = w % 3, hf_ = w / 3;
    const int jglob = cgp * 3 + ct;
    pst8(mt + (((size_t)jglob * 18 + hp) * 64 + lane) * 8 + hf_ * 4, mrow4(16 * hf_ + 4 * g, ct));
  } else if (w == 6) {
    if (g < 3) {
      const int jglob = cgp * 3 + g;
      pst8(mt + (((size_t)jglob * 18 + 16 + (hp >> 3)) * 64 + ((hp >> 1) & 3) * 16 + n16) * 8 + (hp & 1) * 4, mrow4(32, g));
    }
  } else if (cgp == 0) {
    const int hrow = (hp >> 3) * 16 + n16;
    float o[8];
#pragma unroll
    for (int e8 = 0; e8 < 8; ++e8) {
      const int c = 16 * (e8 >> 2) + 4 * g + (e8 & 3), hh = c >= 18 ? 1 : 0;
      o[e8] = (2 * hp + hh == hrow) ? kvs[hh][324 + c - 18 * hh] : 0.f;
    }
    pst16(zt + ((size_t)hp * 64 + lane) * 8, u32x4{pk2(o[0], o[1]), pk2(o[2], o[3]), pk2(o[4], o[5]), pk2(o[6], o[7])});
    if (g == 0) {
      const bool mine = (2 * hp + 1 == hrow);
      float z4[4];
#pragma unroll
      for (int i = 0; i < 4; ++i) z4[i] = mine ? kvs[1][324 + 14 + i] : 0.f;
      pst8(zt + (((size_t)16 + (hp >> 3)) * 64 + ((hp >> 1) & 3) * 16 + n16) * 8 + (hp & 1) * 4, u32x2{pk2(z4[0], z4[1]), pk2(z4[2], z4[3])});
    }
  }
  TL_END();
}

__global__ __launch_bounds__(576) void k_b(const u16* __restrict__ pq, const u16* __restrict__ mt, const u16* __restrict__ zt, const u16* __restrict__ xr,
                                           u16* __restrict__ x1, float* __restrict__ st TL_PARAM DBG_PARAM) {
  TL_BEGIN();
  __shared__ __attribute__((aligned(16))) char sq[2 * 18432];
  __shared__ float rden[32][33];
  __shared__ float sred[9][32][2];
  const int tid = threadIdx.x, lane = tid & 63;
  const int w = __builtin_amdgcn_readfirstlane(tid >> 6);
  const int xcd = blockIdx.x & 7, idx = blockIdx.x >> 3;
  const int tt = xcd * 8 + (idx >> 2), nt = idx & 3;
  const int g = lane >> 4, t16 = lane & 15;
  RT_BEGIN(); STAMP(0);
  u32x4 pf[4];
#pragma unroll
  for (int i = 0; i < 4; ++i) pf[i] = *(const u32x4*)(pq + ((size_t)tt * 36 + w * 4 + i) * 512 + lane * 8);
  u32x4 zf[9];
  if (w < 4) {
#pragma unroll
    for (int s = 0; s < 9; ++s) zf[s] = *(const u32x4*)(zt + ((size_t)(s < 8 ? (w & 1) * 8 + s : 16 + (w & 1)) * 64 + lane) * 8);
  }
  __builtin_amdgcn_sched_barrier(0);
  u32x2 xres[2];
  const int cb = nt * 9 + w;
#pragma unroll
  for (int m = 0; m < 2; ++m) xres[m] = *(const u32x2*)(xr + (((size_t)(tt * 2 + m) * 18 + (cb >> 1)) * 64 + lane) * 8 + (cb & 1) * 4);
  u32x4 mf[18];
  const u16* mb = mt + ((size_t)(nt * 9 + w) * 18 * 64 + lane) * 8;
#pragma unroll
  for (int s = 0; s < 6; ++s) mf[s] = *(const u32x4*)(mb + (size_t)s * 512);
  __builtin_amdgcn_sched_barrier(0);
#pragma unroll
  for (int i = 0; i < 4; ++i) *(u32x4*)(sq + (w * 4 + i) * 1024 + lane * 16) = pf[i];
  STAMP(1);
  __syncthreads();
  if (w >= 4) {
#pragma unroll
    for (int s = 6; s < 18; ++s) mf[s] = *(const u32x4*)(mb + (size_t)s * 512);
  }
  __builtin_amdgcn_sched_barrier(0);
  if (w < 4) {
    f32x4 d = {0, 0, 0, 0};
#pragma unroll
    for (int s = 0; s < 9; ++s) {
      const u32x4 b = *(const u32x4*)(sq + ((w >> 1) * 18 + (s < 8 ? (w & 1) * 8 + s : 16 + (w & 1))) * 1024 + lane * 16);
      d = MFMA16(__builtin_bit_cast(bf16x8, zf[s]), __builtin_bit_cast(bf16x8, b), d);
    }
#pragma unroll
    for (int i = 0; i < 4; ++i) rden[(w >> 1) * 16 + t16][(w & 1) * 16 + 4 * g + i] = __builtin_amdgcn_rcpf(d[i] + 1e-6f);
#pragma unroll
    for (int s = 6; s < 18; ++s) mf[s] = *(const u32x4*)(mb + (size_t)s * 512);
  }
  __syncthreads();
  STAMP(2);
#pragma unroll
  for (int i = 0; i < 4; ++i) {
    const int f = w * 4 + i, T = f / 18, s = f % 18;
    const float* rd = rden[T * 16 + t16];
    int ha, hb2, hc, hd2;
    if (s < 16) { ha = 2 * s; hb2 = ha; hc = 2 * s + (g > 0 ? 1 : 0); hd2 = 2 * s + 1; }
    else { const int qq = 4 * (s - 16) + g; ha = 4 * qq + 1; hb2 = ha; hc = 4 * qq + 3; hd2 = hc; }
    const float r0 = rd[ha], r1 = rd[hb2], r2 = rd[hc], r3 = rd[hd2];
    const u32x4 v = pf[i];
    *(u32x4*)(sq + f * 1024 + lane * 16) = u32x4{pk2(lo16(v[0]) * r0, hi16(v[0]) * r0), pk2(lo16(v[1]) * r1, hi16(v[1]) * r1), pk2(lo16(v[2]) * r2, hi16(v[2]) * r2), pk2(lo16(v[3]) * r3, hi16(v[3]) * r3)};
  }
  __syncthreads();
  STAMP(3);
  f32x4 acc[2] = {{0, 0, 0, 0}, {0, 0, 0, 0}};
#pragma unroll
  for (int s = 0; s < 18; ++s) {
    const u32x4 b0 = *(const u32x4*)(sq + s * 1024 + lane * 16), b1 = *(const u32x4*)(sq + (18 + s) * 1024 + lane * 16);
    acc[0] = MFMA16(__builtin_bit_cast(bf16x8, mf[s]), __builtin_bit_cast(bf16x8, b0), acc[0]);
    acc[1] = MFMA16(__builtin_bit_cast(bf16x8, mf[s]), __builtin_bit_cast(bf16x8, b1), acc[1]);
  }
  STAMP(4);
#pragma unroll
  for (int m = 0; m < 2; ++m) {
    const unsigned o0 = pk2(acc[m][0] + lo16(xres[m][0]), acc[m][1] + hi16(xres[m][0])), o1 = pk2(acc[m][2] + lo16(xres[m][1]), acc[m][3] + hi16(xres[m][1]));
    pst8(x1 + (((size_t)(tt * 2 + m) * 18 + (cb >> 1)) * 64 + lane) * 8 + (cb & 1) * 4, u32x2{o0, o1});
    const float a0 = lo16(o0), a1 = hi16(o0), a2 = lo16(o1), a3 = hi16(o1);
    float sm = (a0 + a1) + (a2 + a3), sq2 = (a0 * a0 + a1 * a1) + (a2 * a2 + a3 * a3);
    sm += __shfl_xor(sm, 16); sq2 += __shfl_xor(sq2, 16);
    sm += __shfl_xor(sm, 32); sq2 += __shfl_xor(sq2, 32);
    if (g == 0) { sred[w][m * 16 + t16][0] = sm; sred[w][m * 16 + t16][1] = sq2; }
  }
  __syncthreads();
  if (tid < 64) {
    const int t = tid >> 1, c = tid & 1;
    float a = 0.f;
#pragma unroll
    for (int i = 0; i < 9; ++i) a += sred[i][t][c];
    pst4(st + ((size_t)(tt * 32 + t) * 4 + nt) * 2 + c, a);
  }
  STAMP(5);
  RT_END();
  TL_END();
}

#define FLUSHL2()
extern "C" void kernel_launch(void* const* d_in, const int* in_sizes, int n_in, void* d_out, int out_size, void* d_ws, size_t ws_size, hipStream_t stream) {
  const u16* x = (const u16*)d_in[0]; const u16* wqkv = (const u16*)d_in[1]; const u16* wo = (const u16*)d_in[2];
  const u16* wg = (const u16*)d_in[4]; const u16* wu = (const u16*)d_in[5]; const u16* wd = (const u16*)d_in[6];
  char* ws = (char*)d_ws; size_t off = 0; auto al = [&](size_t b) { void* p = ws + off; off += (b + 1023) / 1024 * 1024; return p; };
  u16* xr = (u16*)al((size_t)S_ * D_ * 2); u16* xn = (u16*)al((size_t)S_ * D_ * 2); u16* wt = (u16*)al((size_t)2016 * 1024);
  u16* pq = (u16*)al((size_t)S_ * D_ * 2); float* kvp = (float*)al((size_t)16 * 32 * 344 * 4); u16* mt = (u16*)al((size_t)36 * 18 * 1024); u16* zt = (u16*)al(18 * 1024);
  u16* x1 = (u16*)al((size_t)S_ * D_ * 2); float* st = (float*)al((size_t)S_ * 8 * 4); u16* h = (u16*)al((size_t)S_ * F_ * 2); u16* part = (u16*)al((size_t)4 * S_ * D_ * 2);
  unsigned long long* dbg = nullptr;
  (void)hipFuncSetAttribute((const void*)k_a, hipFuncAttributeMaxDynamicSharedMemorySize, A_LDS);
  (void)hipFuncSetAttribute((const void*)k_c, hipFuncAttributeMaxDynamicSharedMemorySize, C_LDS);
  (void)hipFuncSetAttribute((const void*)k_d, hipFuncAttributeMaxDynamicSharedMemorySize, D_LDS);
  for (int l = 0; l < 2; ++l) {
    if (l == 0) k_r<0><<<256 + 224, 576, 0, stream>>>(x, xr, xn, nullptr, wqkv, wt TL_ARG(l * 6 + 0));
    else k_r<1><<<256, 576, 0, stream>>>(part, xr, xn, nullptr, nullptr, nullptr TL_ARG(l * 6 + 0));
    k_a<<<256, 512, A_LDS, stream>>>(xn, wt, pq, kvp TL_ARG(l * 6 + 1) DBG_ARG(2));
    k_a2<<<192, 512, 0, stream>>>(kvp, wo, mt, zt TL_ARG(l * 6 + 2));
    k_b<<<256, 576, 0, stream>>>(pq, mt, zt, xr, x1, st TL_ARG(l * 6 + 3) DBG_ARG(3));
    k_c<<<256, 512, C_LDS, stream>>>(x1, st, wg, wu, h, dbg TL_ARG(l * 6 + 4));
    k_d<<<256, 512, D_LDS, stream>>>(h, wd, x1, part, dbg ? dbg + 256 * 32 : dbg TL_ARG(l * 6 + 5));
  }
  k_r<2><<<256, 576, 0, stream>>>(part, nullptr, nullptr, (u16*)d_out, nullptr, nullptr TL_ARG(12));
}
```

```cpp
#include <hip/hip_runtime.h>
#include <hip/hip_bf16.h>
#include <stdint.h>

#define LAS __attribute__((address_space(3)))
#define GAS __attribute__((address_space(1)))
#define DI __device__ __forceinline__
typedef __attribute__((ext_vector_type(8))) __bf16 bf16x8;
typedef __attribute__((ext_vector_type(2))) __bf16 bf16x2;
typedef __attribute__((ext_vector_type(4))) short s16x4;
typedef __attribute__((ext_vector_type(8))) short s16x8;
typedef __attribute__((ext_vector_type(4))) float f32x4;
typedef __attribute__((ext_vector_type(2))) float f32x2;
typedef __attribute__((ext_vector_type(4))) unsigned u32x4;
typedef __attribute__((ext_vector_type(2))) unsigned u32x2;
typedef unsigned short u16;

constexpr int S_ = 2048, D_ = 576, F_ = 4608;
#define MFMA16(a, b, c) __builtin_amdgcn_mfma_f32_16x16x32_bf16((a), (b), (c), 0, 0, 0)

DI float bf2f(u16 b) { return __uint_as_float((unsigned)b << 16); }
DI u16 f2bf(float f) { __bf16 b = (__bf16)f; return __builtin_bit_cast(u16, b); }
DI unsigned pk2(float a, float b) { f32x2 v = {a, b}; bf16x2 r = __builtin_convertvector(v, bf16x2); return __builtin_bit_cast(unsigned, r); }
DI float lo16(unsigned u) { return __uint_as_float(u << 16); }
DI float hi16(unsigned u) { return __uint_as_float(u & 0xffff0000u); }

DI void glds16(unsigned voff, const void* sbase, unsigned lds) {
  asm volatile("s_mov_b32 m0, %2\n\ts_nop 0\n\tglobal_load_lds_dwordx4 %0, %1" :: "v"(voff), "s"(sbase), "s"(lds) : "memory");
}
#define WAITV(n) asm volatile("s_waitcnt vmcnt(%0)" :: "n"(n) : "memory")
#define WAITL0() asm volatile("s_waitcnt lgkmcnt(0)" ::: "memory")
#define BAR() do { asm volatile("" ::: "memory"); __builtin_amdgcn_s_barrier(); asm volatile("" ::: "memory"); } while (0)

DI bf16x8 ld_frag(LAS char* p) { return __builtin_bit_cast(bf16x8, *(LAS u32x4*)p); }
#define SB0() __builtin_amdgcn_sched_barrier(0)
DI bf16x8 ld_tr(LAS char* p0, LAS char* p1) {
  s16x4 a = __builtin_amdgcn_ds_read_tr16_b64_v4i16((LAS s16x4*)p0);
  s16x4 b = __builtin_amdgcn_ds_read_tr16_b64_v4i16((LAS s16x4*)p1);
  s16x8 r = __builtin_shufflevector(a, b, 0, 1, 2, 3, 4, 5, 6, 7);
  return __builtin_bit_cast(bf16x8, r);
}

template <int I> struct IC { static constexpr int value = I; constexpr operator int() const { return I; } };
template <int B, int E, class Fn> DI void static_for(Fn&& f) { if constexpr (B < E) { f(IC<B>{}); static_for<B + 1, E>(f); } }
#define AMF(acc, a, b) asm volatile("v_mfma_f32_16x16x32_bf16 %0, %1, %2, %0" : "+v"(acc) : "v"(a), "v"(b))
#define AMF0(acc, a, b) asm volatile("v_mfma_f32_16x16x32_bf16 %0, %1, %2, 0" : "=&v"(acc) : "v"(a), "v"(b))
#define PIN(x) asm volatile("" : "+v"(x))
DI void pst16(void* p, u32x4 v) { asm volatile("global_store_dwordx4 %0, %1, off sc1\n\ts_nop 1" :: "v"(p), "v"(v) : "memory"); }
DI void pst8(void* p, u32x2 v) { asm volatile("global_store_dwordx2 %0, %1, off sc1" :: "v"(p), "v"(v) : "memory"); }
DI void pst4(void* p, float v) { asm volatile("global_store_dword %0, %1, off sc1" :: "v"(p), "v"(v) : "memory"); }
DI void gld16(u32x4& r, unsigned voff, const void* sbase) { asm volatile("global_load_dwordx4 %0, %1, %2" : "=v"(r) : "v"(voff), "s"(sbase) : "memory"); }

constexpr int XSLOT = 18432;
constexpr int C_STATS = 8 * XSLOT;
constexpr int C_LDS = C_STATS + 304 * 16;
constexpr int C_LA = 4, C_T1 = 5;
constexpr int c_ext(int s) { return s == 1 ? 5 : (s == 13 || s == 14 || s == 15) ? 2 : 0; }
constexpr int c_wc(int s) { return s + C_LA < 18 ? 2 : 0; }
constexpr int c_wait(int s) {
  int n = 0;
  if (s < C_LA) { n = 2 * (C_LA - 1 - s); for (int j = 0; j < s; ++j) n += c_wc(j) + c_ext(j); }
  else { n = c_ext(s - C_LA); for (int j = s - C_LA + 1; j < s; ++j) n += c_wc(j) + c_ext(j); }
  return n;
}
extern "C" __global__ __launch_bounds__(512, 2) void k_c(const u16* __restrict__ x1, const float* __restrict__ st, const u16* __restrict__ wgt, const float* __restrict__ cs,
                                                        u16* __restrict__ h) {
  extern __shared__ __attribute__((aligned(16))) char smem_[];
  LAS char* smem = (LAS char*)smem_;
  const int tid = threadIdx.x, lane = tid & 63;
  const int w = __builtin_amdgcn_readfirstlane(tid >> 6);
  const int xcd = blockIdx.x & 7, idx = blockIdx.x >> 3;
  const int start = (63 * xcd + 1) >> 1, cnt = ((63 * (xcd + 1) + 1) >> 1) - start;
  if (idx >= cnt) return;
  const int L = start + idx, slice = L / 7, range = L % 7;
  const int ntile = range < 2 ? 19 : 18, tile0 = range < 2 ? 19 * range : 38 + 18 * (range - 2);
  const unsigned ldsb = (unsigned)(size_t)smem;
  const int g = lane >> 4, t16 = lane & 15;
  const int fb = slice * 8 + w;
  const unsigned vlin = lane * 16;

  u32x4 s0, s1, c0, c1, c2, c3, c4, c5;
  { const unsigned so = (unsigned)min(tid, ntile * 16 - 1) * 32u; const float* sb = st + (size_t)tile0 * 16 * 8;
    asm volatile("global_load_dwordx4 %0, %1, %2" : "=v"(s0) : "v"(so), "s"(sb) : "memory");
    asm volatile("global_load_dwordx4 %0, %1, %2 offset:16" : "=v"(s1) : "v"(so), "s"(sb) : "memory");
    const unsigned co = g * 16; const float* cb = cs + fb * 16;
    gld16(c0, co, cb); gld16(c1, co, cb + F_); gld16(c2, co, cb + 2 * F_); gld16(c3, co, cb + 3 * F_); gld16(c4, co, cb + 4 * F_); gld16(c5, co, cb + 5 * F_); }

  const char* xb = (const char*)x1 + (size_t)tile0 * XSLOT;
  const char* wb = (const char*)wgt + (size_t)fb * 2 * 18432;
  bf16x8 wg[18], wu[18];
  auto issueW = [&](int s) {
    asm volatile("global_load_dwordx4 %0, %1, %2" : "=v"(wg[s]) : "v"(vlin), "s"(wb + s * 1024) : "memory");
    asm volatile("global_load_dwordx4 %0, %1, %2" : "=v"(wu[s]) : "v"(vlin), "s"(wb + 18432 + s * 1024) : "memory");
  };
  auto issueXG = [&](int grp) {
    const int s = 8 * grp + w;
    if (s < 18) {
#pragma unroll
      for (int T = 0; T < C_T1; ++T) glds16(vlin, xb + T * XSLOT + s * 1024, ldsb + T * XSLOT + s * 1024);
    }
  };
  const unsigned vp0 = vlin + w * 1024, vp1 = vp0 + 8192, vp2 = vp0 + 16384;
  const unsigned ldw = ldsb + w * 1024;
  auto load_tile = [&](int T) {
    const char* src = xb + min(T, ntile - 1) * XSLOT; const unsigned dst = ldw + (T & 7) * XSLOT;
    glds16(vp0, src, dst); glds16(vp1, src, dst + 8192);
    if (w < 2) glds16(vp2, src, dst + 16384);
  };
  issueXG(0);
  BAR();
#pragma unroll
  for (int s = 0; s < C_LA; ++s) issueW(s);

  f32x4 a1g[C_T1], a1u[C_T1], cg, cu;
  LAS char* xl = smem + lane * 16;
  bf16x8 xr[6];
  constexpr int PD = 4;
  {
    asm volatile("s_waitcnt vmcnt(%8)" : "+v"(s0), "+v"(s1), "+v"(c0), "+v"(c1), "+v"(c2), "+v"(c3), "+v"(c4), "+v"(c5) : "n"(2 * C_LA) : "memory");
    float sum = __uint_as_float(s0[0]) + __uint_as_float(s0[2]) + __uint_as_float(s1[0]) + __uint_as_float(s1[2]);
    float sq = __uint_as_float(s0[1]) + __uint_as_float(s0[3]) + __uint_as_float(s1[1]) + __uint_as_float(s1[3]);
    float mean = sum * (1.0f / D_), var = sq * (1.0f / D_) - mean * mean;
    const float irs2 = fmaxf(var, 0.f) + 1e-5f, rs = __builtin_amdgcn_rsqf(irs2);
    const float c1_ = -2.0f * 0.7978845608028654f * 1.4426950408889634f;
    if (tid < 304) *(LAS f32x4*)(smem + C_STATS + tid * 16) = f32x4{-mean, c1_ * rs, c1_ * 0.044715f * rs * rs * rs, irs2};
    cg = __builtin_bit_cast(f32x4, c0) + __builtin_bit_cast(f32x4, c2) + __builtin_bit_cast(f32x4, c4);
    cu = __builtin_bit_cast(f32x4, c1) + __builtin_bit_cast(f32x4, c3) + __builtin_bit_cast(f32x4, c5);
    WAITL0(); BAR();
#pragma unroll
    for (int i = 0; i < PD; ++i) xr[i] = ld_frag(xl + (i % C_T1) * XSLOT + (i / C_T1) * 1024);
  }
  static_for<0, 18>([&](auto S_) {
    constexpr int s = S_;
    SB0();
    asm volatile("s_waitcnt vmcnt(%2)" : "+v"(wg[s]), "+v"(wu[s]) : "n"(c_wait(s)) : "memory");
    if constexpr (s == 16) WAITV(4);
    if constexpr (s == 6 || s == 13 || s == 16) BAR();
    static_for<0, C_T1>([&](auto T_) {
      constexpr int T = T_, i = C_T1 * s + T, in = i + PD;
      if constexpr (s == 0) AMF0(a1g[T], wg[s], xr[i % 6]); else AMF(a1g[T], wg[s], xr[i % 6]);
      SB0();
      if constexpr (in < 18 * C_T1) xr[in % 6] = ld_frag(xl + (in % C_T1) * XSLOT + (in / C_T1) * 1024);
      else xr[in % 6] = ld_frag(xl + C_T1 * XSLOT + (in - 18 * C_T1) * 1024);
      if constexpr (T == 0) { if constexpr (s + C_LA < 18) issueW(s + C_LA); }
      if constexpr (T == 2) {
        if constexpr (s == 1) issueXG(1);
        if constexpr (s == 8) issueXG(2);
        if constexpr (s >= 13 && s <= 15) load_tile(s - 8);
      }
      SB0();
      if constexpr (s == 0) AMF0(a1u[T], wu[s], xr[i % 6]); else AMF(a1u[T], wu[s], xr[i % 6]);
      SB0();
    });
  });
  BAR();
  load_tile(8);

  constexpr size_t HT = (size_t)(F_ / 32) * 1024;
  const char* hb = (const char*)h + ((size_t)tile0 * (F_ / 32) + slice * 4 + (w >> 1)) * 1024 + (w & 1) * 512;
  const unsigned hvo = (lane >> 5) * (unsigned)HT + ((lane >> 4) & 1) * 256 + t16 * 16;
  auto store2 = [&](int T, u32x2 a, u32x2 b) {
    auto r0 = __builtin_amdgcn_permlane32_swap(a[0], b[0], false, false);
    auto r1 = __builtin_amdgcn_permlane32_swap(a[1], b[1], false, false);
    auto q0 = __builtin_amdgcn_permlane16_swap(r0[0], r0[1], false, false);
    auto q1 = __builtin_amdgcn_permlane16_swap(r1[0], r1[1], false, false);
    const u32x4 v = {q0[0], q1[0], q0[1], q1[1]};
    asm volatile("global_store_dwordx4 %0, %1, %2 sc1\n\ts_nop 1" :: "v"(hvo), "v"(v), "s"(hb + (size_t)T * HT) : "memory");
  };
  auto epi = [&](const f32x4& ag, const f32x4& au, int T) -> u32x2 {
    f32x4 sv = *(LAS f32x4*)(smem + C_STATS + (T * 16 + t16) * 16);
    float hv[4];
#pragma unroll
    for (int i = 0; i < 4; ++i) {
      float tg = __builtin_fmaf(sv[0], cg[i], ag[i]), tu = __builtin_fmaf(sv[0], cu[i], au[i]);
      float y = __builtin_fmaf(tg * tg, sv[2], sv[1]) * tg;
      float e = __builtin_amdgcn_exp2f(y);
      float r = __builtin_amdgcn_rcpf(__builtin_fmaf(e, sv[3], sv[3]));
      hv[i] = tg * tu * r;
    }
    return u32x2{pk2(hv[0], hv[1]), pk2(hv[2], hv[3])};
  };
  asm volatile("s_nop 7\n\ts_nop 1" : "+v"(a1g[0]), "+v"(a1g[1]), "+v"(a1g[2]), "+v"(a1g[3]), "+v"(a1g[4]), "+v"(a1u[0]), "+v"(a1u[1]), "+v"(a1u[2]), "+v"(a1u[3]), "+v"(a1u[4]));
  { const u32x2 o0 = epi(a1g[0], a1u[0], 0), o1 = epi(a1g[1], a1u[1], 1); store2(0, o0, o1); }
  { const u32x2 o0 = epi(a1g[2], a1u[2], 2), o1 = epi(a1g[3], a1u[3], 3); store2(2, o0, o1); }
  const int ph = w >> 2;
  u32x2 oprev = {0, 0};
  auto tile = [&](int T, f32x4& ag, f32x4& au, const f32x4& pg, const f32x4& pu, bool second) {
    LAS char* xt = xl + (T & 7) * XSLOT;
    LAS char* xn = xl + ((T + 1) & 7) * XSLOT;
    f32x4 sv = *(LAS f32x4*)(smem + C_STATS + ((T - 1) * 16 + t16) * 16);
    float tg[2], tu[2], y[2], e[2], m[2], r[2];
    unsigned o[2] = {0, 0};
    const char* nsrc = xb + min(T + 4, ntile - 1) * XSLOT; const unsigned ndst = ldw + ((T + 4) & 7) * XSLOT;
    auto eslot = [&](int q) {
      if (q < 0 || q >= 24) return;
      const int pr = q / 12, op = q % 12, ia = 2 * pr, ib = 2 * pr + 1;
      switch (op) {
        case 0: tg[0] = __builtin_fmaf(sv[0], cg[ia], pg[ia]); tg[1] = __builtin_fmaf(sv[0], cg[ib], pg[ib]); PIN(tg[0]); PIN(tg[1]); break;
        case 1: tu[0] = __builtin_fmaf(sv[0], cu[ia], pu[ia]); tu[1] = __builtin_fmaf(sv[0], cu[ib], pu[ib]); PIN(tu[0]); PIN(tu[1]); break;
        case 2: y[0] = tg[0] * tg[0]; y[1] = tg[1] * tg[1]; PIN(y[0]); PIN(y[1]); break;
        case 3: y[0] = __builtin_fmaf(y[0], sv[2], sv[1]); y[1] = __builtin_fmaf(y[1], sv[2], sv[1]); PIN(y[0]); PIN(y[1]); break;
        case 4: y[0] = y[0] * tg[0]; y[1] = y[1] * tg[1]; PIN(y[0]); PIN(y[1]); break;
        case 5: e[0] = __builtin_amdgcn_exp2f(y[0]); m[0] = tg[0] * tu[0]; PIN(e[0]); PIN(m[0]); break;
        case 6: e[1] = __builtin_amdgcn_exp2f(y[1]); m[1] = tg[1] * tu[1]; PIN(e[1]); PIN(m[1]); break;
        case 7: e[0] = __builtin_fmaf(e[0], sv[3], sv[3]); e[1] = __builtin_fmaf(e[1], sv[3], sv[3]); PIN(e[0]); PIN(e[1]); break;
        case 8: r[0] = __builtin_amdgcn_rcpf(e[0]); PIN(r[0]); break;
        case 9: r[1] = __builtin_amdgcn_rcpf(e[1]); PIN(r[1]); break;
        case 10: m[0] = m[0] * r[0]; m[1] = m[1] * r[1]; PIN(m[0]); PIN(m[1]); break;
        default: o[pr] = pk2(m[0], m[1]); PIN(o[pr]); break;
      }
    };
    SB0();
#pragma unroll
    for (int s = 0; s < 18; ++s) {
      if (s == 8) { WAITV(4); BAR(); SB0(); }
      if (s == 0) AMF0(ag, wg[0], xr[0]); else AMF(ag, wg[s], xr[s % 6]);
      SB0();
      if (s + PD < 18) xr[(s + PD) % 6] = ld_frag(xt + (s + PD) * 1024); else xr[(s + PD) % 6] = ld_frag(xn + (s + PD - 18) * 1024);
      eslot(2 * s - 4);
      SB0();
      if (s == 0) AMF0(au, wu[0], xr[0]); else AMF(au, wu[s], xr[s % 6]);
      SB0();
      eslot(2 * s - 3);
      if (s >= 9 && s <= 14) { const int pc = (s - 9) >> 1; if (((s - 9) & 1) == ph && (pc < 2 || w < 2)) glds16(pc == 0 ? vp0 : pc == 1 ? vp1 : vp2, nsrc, ndst + pc * 8192); }
      if (s == 15) { if (second) store2(T - 2, oprev, u32x2{o[0], o[1]}); else oprev = u32x2{o[0], o[1]}; }
      SB0();
    }
  };
  f32x4 Ag, Au, Bg = a1g[C_T1 - 1], Bu = a1u[C_T1 - 1];
  bool pend;
  for (int T = C_T1;; T += 2) {
    tile(T, Ag, Au, Bg, Bu, false);
    if (T + 1 >= ntile) { asm volatile("s_nop 7\n\ts_nop 1" : "+v"(Ag), "+v"(Au)); Bg = Ag; Bu = Au; pend = true; break; }
    tile(T + 1, Bg, Bu, Ag, Au, true);
    if (T + 2 >= ntile) { asm volatile("s_nop 7\n\ts_nop 1" : "+v"(Bg), "+v"(Bu)); pend = false; break; }
  }
  {
    const u32x2 o = epi(Bg, Bu, ntile - 1);
    if (pend) store2(ntile - 2, oprev, o);
    else if (lane < 32 || true) {
      auto r0 = __builtin_amdgcn_permlane32_swap(o[0], o[0], false, false);
      auto r1 = __builtin_amdgcn_permlane32_swap(o[1], o[1], false, false);
      auto q0 = __builtin_amdgcn_permlane16_swap(r0[0], r0[1], false, false);
      auto q1 = __builtin_amdgcn_permlane16_swap(r1[0], r1[1], false, false);
      if (lane < 32) { const u32x4 v = {q0[0], q1[0], q0[1], q1[1]}; pst16((char*)hb + (size_t)(ntile - 1) * HT + hvo, v); }
    }
  }
}

constexpr int D_STAGE = 2 * 18432;
constexpr int D_LDS = 4 * D_STAGE;
extern "C" __global__ __launch_bounds__(512, 2) void k_d(const u16* __restrict__ h, const u16* __restrict__ wdt, const u16* __restrict__ x1, u16* __restrict__ ex, unsigned* __restrict__ cnt,
                                                        u16* __restrict__ xr, float* __restrict__ st, u16* __restrict__ outp) {
  extern __shared__ __attribute__((aligned(16))) char smem_[];
  LAS char* smem = (LAS char*)smem_;
  const int tid = threadIdx.x, lane = tid & 63;
  const int w = __builtin_amdgcn_readfirstlane(tid >> 6);
  const int xcd = blockIdx.x & 7, idx = blockIdx.x >> 3;
  const int ks = xcd & 3, tt = (xcd >> 2) * 8 + (idx >> 2), nt = idx & 3;
  const int mq = w & 3, kh = w >> 2;
  const unsigned ldsb = (unsigned)(size_t)smem, vlin = lane * 16;
  const int g = lane >> 4, t16 = lane & 15;
  const int sg0 = ks * 36 + kh * 18;
  const char* wb = (const char*)wdt + ((size_t)nt * 144 + sg0) * 9216 + mq * 1024;
  const unsigned ldw = ldsb + kh * 18432 + mq * 1024;
  auto stageW = [&](int it, int n) {
    if (n < 4 || mq < 2) glds16(vlin, wb + (size_t)it * 18432 + n * 4096, ldw + (it & 3) * D_STAGE + n * 4096);
  };
  const char* hb = (const char*)h + ((size_t)(tt * 8 + 2 * mq) * (F_ / 32) + sg0) * 1024;
  u32x4 hr[3][4];
  auto loadH = [&](int it, int n, u32x4& r) { const int u = n >> 1, m = n & 1; gld16(r, vlin, hb + (size_t)m * (F_ / 32) * 1024 + (it * 2 + u) * 1024); };
#pragma unroll
  for (int n = 0; n < 5; ++n) stageW(0, n);
#pragma unroll
  for (int n = 0; n < 4; ++n) loadH(0, n, hr[0][n]);
  BAR();
#pragma unroll
  for (int n = 0; n < 5; ++n) stageW(1, n);
#pragma unroll
  for (int n = 0; n < 4; ++n) loadH(1, n, hr[1][n]);
#pragma unroll
  for (int n = 0; n < 5; ++n) stageW(2, n);
  f32x4 acc[2][9];
  LAS char* wl = smem + kh * 18432 + lane * 16;
  constexpr int WPD = 3;
  bf16x8 wf[6];
  asm volatile("s_waitcnt vmcnt(12)" : "+v"(hr[0][0]), "+v"(hr[0][1]), "+v"(hr[0][2]), "+v"(hr[0][3]) :: "memory");
  BAR();
#pragma unroll
  for (int i = 0; i < WPD; ++i) wf[i] = ld_frag(wl + i * 1024);
  auto iter = [&](auto IT_, int it) {
    constexpr int r3 = IT_;
    u32x4(&r)[4] = hr[r3];
    LAS char* wc = wl + (it & 3) * D_STAGE;
    LAS char* wn = wl + ((it + 1) & 3) * D_STAGE;
    const bool first = (r3 == 0) && (it == 0);
    SB0();
    static_for<0, 18>([&](auto I_) {
      constexpr int i = I_, u = i / 9, j = i % 9;
      if constexpr (i == 18 - WPD) {
        if (it < 8) {
          constexpr int NH = (18 - WPD - 10 + 1) / 2;
          if (it < 6) asm volatile("s_waitcnt vmcnt(%0) lgkmcnt(0)" :: "n"(4 + NH) : "memory");
          else if (it == 6) asm volatile("s_waitcnt vmcnt(%0) lgkmcnt(0)" :: "n"(NH) : "memory");
          else asm volatile("s_waitcnt vmcnt(0) lgkmcnt(0)" ::: "memory");
          BAR();
        }
        { u32x4(&rn)[4] = hr[(r3 + 1) % 3]; asm volatile("" : "+v"(rn[0]), "+v"(rn[1]), "+v"(rn[2]), "+v"(rn[3])); }
        SB0();
      }
      if constexpr (i + WPD < 18) wf[(i + WPD) % 6] = ld_frag(wc + (i + WPD) * 1024);
      else wf[(i + WPD) % 6] = ld_frag(wn + (i + WPD - 18) * 1024);
      AMF(acc[0][j], wf[i % 6], r[u * 2 + 0]); AMF(acc[1][j], wf[i % 6], r[u * 2 + 1]);
      if constexpr (i <= 8 && (i & 1) == 0) { if (it + 3 < 9) stageW(it + 3, i / 2); }
      if constexpr (i == 10 || i == 12 || i == 14 || i == 16) { if (it + 2 < 9) loadH(it + 2, (i - 10) / 2, hr[(r3 + 2) % 3][(i - 10) / 2]); }
      SB0();
    });
    (void)first;
  };
#pragma unroll
  for (int m = 0; m < 2; ++m)
#pragma unroll
    for (int j = 0; j < 9; ++j) { acc[m][j] = f32x4{0, 0, 0, 0}; }
  asm volatile("s_nop 1" : "+v"(acc[0][0]), "+v"(acc[0][1]), "+v"(acc[0][2]), "+v"(acc[0][3]), "+v"(acc[0][4]), "+v"(acc[0][5]), "+v"(acc[0][6]), "+v"(acc[0][7]), "+v"(acc[0][8]),
               "+v"(acc[1][0]), "+v"(acc[1][1]), "+v"(acc[1][2]), "+v"(acc[1][3]), "+v"(acc[1][4]), "+v"(acc[1][5]), "+v"(acc[1][6]), "+v"(acc[1][7]), "+v"(acc[1][8]));
#pragma unroll 1
  for (int it = 0; it < 9; it += 3) { iter(IC<0>{}, it); iter(IC<1>{}, it + 1); iter(IC<2>{}, it + 2); }
  asm volatile("s_nop 7\n\ts_nop 1" : "+v"(acc[0][8]), "+v"(acc[1][8]) :: "memory");
  const int Tg = tt * 8 + 2 * mq + kh;
  const bool own = mq == ks;
  const int cb0 = nt * 9;
  unsigned xres[9][2];
  if (own) {
    const char* xrow = (const char*)x1 + (size_t)Tg * 18432 + (g >> 1) * 256 + t16 * 16 + (g & 1) * 8;
#pragma unroll
    for (int j = 0; j < 9; ++j) { const int cb = cb0 + j; const u32x2 t = *(const GAS u32x2*)(xrow + (cb >> 1) * 1024 + (cb & 1) * 512); xres[j][0] = t[0]; xres[j][1] = t[1]; }
  }
  LAS f32x4* red = (LAS f32x4*)(smem + D_STAGE);
  f32x4 vv[9];
  auto fin = [&](f32x4 (&keep)[9], f32x4 (&send)[9]) {
#pragma unroll
    for (int j = 0; j < 9; ++j) red[(w * 9 + j) * 64 + lane] = send[j];
    WAITL0(); BAR();
#pragma unroll
    for (int j = 0; j < 9; ++j) vv[j] = keep[j] + red[((w ^ 4) * 9 + j) * 64 + lane];
  };
  if (kh == 0) fin(acc[0], acc[1]); else fin(acc[1], acc[0]);
  char* exb = (char*)ex + ((size_t)((tt * 4 + nt) * 4 + mq) * 4) * 9216 + kh * 4608;
  if (!own) {
    char* p = exb + ks * 9216;
#pragma unroll
    for (int j = 0; j < 8; j += 2) pst16(p + (j >> 1) * 1024 + lane * 16, u32x4{pk2(vv[j][0], vv[j][1]), pk2(vv[j][2], vv[j][3]), pk2(vv[j + 1][0], vv[j + 1][1]), pk2(vv[j + 1][2], vv[j + 1][3])});
    pst8(p + 4096 + lane * 8, u32x2{pk2(vv[8][0], vv[8][1]), pk2(vv[8][2], vv[8][3])});
  }
  WAITV(0); BAR();
  unsigned* cw = cnt + (tt * 4 + nt) * 32;
  if (tid == 0) __hip_atomic_fetch_add(cw, 1u, __ATOMIC_RELAXED, __HIP_MEMORY_SCOPE_AGENT);
  if (own) {
    for (int spin = 0; spin < (1 << 16); ++spin) {
      unsigned v;
      asm volatile("global_load_dword %0, %1, %2 sc1\n\ts_waitcnt vmcnt(0)" : "=v"(v) : "v"(0u), "s"(cw) : "memory");
      if (__builtin_amdgcn_readfirstlane(v) >= 4u) break;
      __builtin_amdgcn_s_sleep(1);
    }
    u32x4 pa[3][4]; u32x2 pb[3];
#pragma unroll
    for (int i = 0; i < 3; ++i) {
      const char* p = exb + ((ks + 1 + i) & 3) * 9216;
#pragma unroll
      for (int j = 0; j < 4; ++j) asm volatile("global_load_dwordx4 %0, %1, %2 sc1" : "=v"(pa[i][j]) : "v"(lane * 16), "s"(p + j * 1024) : "memory");
      asm volatile("global_load_dwordx2 %0, %1, %2 sc1" : "=v"(pb[i]) : "v"(lane * 8), "s"(p + 4096) : "memory");
    }
    asm volatile("s_waitcnt vmcnt(0)" : "+v"(pa[0][0]), "+v"(pa[0][1]), "+v"(pa[0][2]), "+v"(pa[0][3]), "+v"(pa[1][0]), "+v"(pa[1][1]), "+v"(pa[1][2]), "+v"(pa[1][3]),
                 "+v"(pa[2][0]), "+v"(pa[2][1]), "+v"(pa[2][2]), "+v"(pa[2][3]), "+v"(pb[0]), "+v"(pb[1]), "+v"(pb[2]) :: "memory");
    u32x2 o[9];
    float sm = 0.f, sq = 0.f;
#pragma unroll
    for (int j = 0; j < 9; ++j) {
      f32x4 v = vv[j];
      v[0] += lo16(xres[j][0]); v[1] += hi16(xres[j][0]); v[2] += lo16(xres[j][1]); v[3] += hi16(xres[j][1]);
#pragma unroll
      for (int i = 0; i < 3; ++i) {
        const unsigned a = j < 8 ? pa[i][j >> 1][(j & 1) * 2] : pb[i][0], b = j < 8 ? pa[i][j >> 1][(j & 1) * 2 + 1] : pb[i][1];
        v[0] += lo16(a); v[1] += hi16(a); v[2] += lo16(b); v[3] += hi16(b);
      }
      o[j] = u32x2{pk2(v[0], v[1]), pk2(v[2], v[3])};
      const float a0 = lo16(o[j][0]), a1 = hi16(o[j][0]), a2 = lo16(o[j][1]), a3 = hi16(o[j][1]);
      sm += (a0 + a1) + (a2 + a3); sq += (a0 * a0 + a1 * a1) + (a2 * a2 + a3 * a3);
    }
    if (outp) {
      u16* orow = outp + (size_t)(Tg * 16 + t16) * D_ + nt * 144 + 4 * g;
#pragma unroll
      for (int j = 0; j < 9; ++j) *(GAS u32x2*)(orow + 16 * j) = o[j];
    } else {
      u16* prow = xr + ((size_t)Tg * 18 * 64 + lane) * 8;
      if ((nt & 1) == 0) {
#pragma unroll
        for (int j = 0; j < 8; j += 2) pst16(prow + ((cb0 + j) >> 1) * 512, u32x4{o[j][0], o[j][1], o[j + 1][0], o[j + 1][1]});
        pst8(prow + ((cb0 + 8) >> 1) * 512, o[8]);
      } else {
        pst8(prow + (cb0 >> 1) * 512 + 4, o[0]);
#pragma unroll
        for (int j = 1; j < 9; j += 2) pst16(prow + ((cb0 + j) >> 1) * 512, u32x4{o[j][0], o[j][1], o[j + 1][0], o[j + 1][1]});
      }
      sm += __shfl_xor(sm, 16); sq += __shfl_xor(sq, 16);
      sm += __shfl_xor(sm, 32); sq += __shfl_xor(sq, 32);
      if (g == 0) pst8(st + ((size_t)(Tg * 16 + t16) * 4 + nt) * 2, u32x2{__float_as_uint(sm), __float_as_uint(sq)});
    }
  }
}

DI void prep_x(const u16* __restrict__ src, u16* __restrict__ xr, float* __restrict__ st, unsigned* __restrict__ cnt, float (*red)[8][2]) {
  const int tid = threadIdx.x, lane = tid & 63, w = tid >> 6;
  if (blockIdx.x == 0 && tid < 160) pst4(cnt + tid * 32, 0.f);
  const int t8 = tid & 7, cg = tid >> 3, c32 = cg >> 2, g = cg & 3;
  const int t = blockIdx.x * 8 + t8;
  const size_t ffo = (((size_t)(t >> 4) * 18 + c32) * 64 + g * 16 + (t & 15)) * 8;
  const u32x2 a = *(const u32x2*)(src + (size_t)t * D_ + 32 * c32 + 4 * g), b = *(const u32x2*)(src + (size_t)t * D_ + 32 * c32 + 16 + 4 * g);
  pst16(xr + ffo, u32x4{a[0], a[1], b[0], b[1]});
  const float v[8] = {lo16(a[0]), hi16(a[0]), lo16(a[1]), hi16(a[1]), lo16(b[0]), hi16(b[0]), lo16(b[1]), hi16(b[1])};
  float sm = 0.f, sq = 0.f;
#pragma unroll
  for (int e = 0; e < 8; ++e) { sm += v[e]; sq = __builtin_fmaf(v[e], v[e], sq); }
#pragma unroll
  for (int o = 8; o < 64; o <<= 1) { sm += __shfl_xor(sm, o); sq += __shfl_xor(sq, o); }
  if (lane < 8) { red[w][lane][0] = sm; red[w][lane][1] = sq; }
  __syncthreads();
  if (tid < 32) {
    float s0 = 0.f, s1 = 0.f;
    if (cg == 0) {
#pragma unroll
      for (int i = 0; i < 9; ++i) { s0 += red[i][t8][0]; s1 += red[i][t8][1]; }
    }
    pst8(st + ((size_t)t * 4 + cg) * 2, u32x2{__float_as_uint(s0), __float_as_uint(s1)});
  }
}
DI void retile_qkv(int bq, const u16* __restrict__ wqkv, u16* __restrict__ wt, float* __restrict__ cwq, float (*red)[8][2]) {
  const int tid = threadIdx.x, lane = tid & 63, w = tid >> 6;
  const int f = bq * 9 + w;
  const int s = f % 18, j = (f / 18) % 7, hp = f / 126;
  const int n16 = lane & 15, g = lane >> 4;
  int col; bool valid = true;
  if (j < 6) col = (j >> 1) * 576 + 36 * hp + (j & 1) * 16 + n16;
  else { const int sec = n16 >> 2; valid = sec < 3; col = (valid ? sec : 0) * 576 + 36 * hp + 32 + (n16 & 3); }
  unsigned v[8];
  float sm = 0.f;
#pragma unroll
  for (int e = 0; e < 8; ++e) { const int k = 32 * s + 16 * (e >> 2) + 4 * g + (e & 3); v[e] = valid ? wqkv[(size_t)k * 1728 + col] : 0; }
#pragma unroll
  for (int e = 0; e < 8; ++e) sm += bf2f((u16)v[e]);
  pst16(wt + ((size_t)f * 64 + lane) * 8, u32x4{v[0] | (v[1] << 16), v[2] | (v[3] << 16), v[4] | (v[5] << 16), v[6] | (v[7] << 16)});
  sm += __shfl_xor(sm, 16); sm += __shfl_xor(sm, 32);
  float* r = &red[0][0][0];
  if (g == 0) r[w * 16 + n16] = sm;
  __syncthreads();
  if (tid < 16) {
    float a = 0.f;
#pragma unroll
    for (int i = 0; i < 9; ++i) a += r[i * 16 + tid];
    pst4(cwq + (size_t)((bq & 1) * 112 + (bq >> 1)) * 16 + tid, a);
  }
}

#define NTL(p) __builtin_nontemporal_load(p)
DI void retile_gu(int u, const u16* __restrict__ wg, const u16* __restrict__ wu, u16* __restrict__ wgt, float* __restrict__ cs, LAS char* sm) {
  const int tid = threadIdx.x, lane = tid & 63, w = tid >> 6;
  const int mat = u / 216, rem = u % 216, cb = rem / 3, k3 = rem % 3;
  const char* src = (const char*)(mat ? wu : wg) + (size_t)(192 * k3) * (F_ * 2) + cb * 128;
  u32x4 v[3];
#pragma unroll
  for (int i = 0; i < 3; ++i) { const int q = tid + 512 * i; v[i] = NTL((const u32x4*)(src + (size_t)(q >> 3) * (F_ * 2) + (q & 7) * 16)); }
#pragma unroll
  for (int i = 0; i < 3; ++i) { const int q = tid + 512 * i, r = q >> 3, c = q & 7; *(LAS u32x4*)(sm + r * 128 + ((c ^ (2 * ((r >> 1) & 3))) * 16)) = v[i]; }
  __syncthreads();
  const int n16 = lane & 15, g = lane >> 4, q = n16 >> 2, p = lane & 3;
  LAS float* csum = (LAS float*)(sm + 24576);
#pragma unroll
  for (int i = 0; i < 3; ++i) {
    const int f = 3 * w + i, sl = f >> 2, fbl = f & 3;
    const int r0 = 32 * sl + 8 * g + q, r1 = r0 + 4;
    LAS char* a0 = sm + r0 * 128 + (((2 * fbl + (p >> 1)) ^ (2 * ((r0 >> 1) & 3))) * 16) + 8 * (p & 1);
    LAS char* a1 = sm + r1 * 128 + (((2 * fbl + (p >> 1)) ^ (2 * ((r1 >> 1) & 3))) * 16) + 8 * (p & 1);
    const u32x4 fr = __builtin_bit_cast(u32x4, ld_tr(a0, a1));
    pst16(wgt + ((((size_t)(cb * 4 + fbl) * 2 + mat) * 18 + 6 * k3 + sl) * 64 + lane) * 8, fr);
    float s = ((lo16(fr[0]) + hi16(fr[0])) + (lo16(fr[1]) + hi16(fr[1]))) + ((lo16(fr[2]) + hi16(fr[2])) + (lo16(fr[3]) + hi16(fr[3])));
    s += __shfl_xor(s, 16); s += __shfl_xor(s, 32);
    if (g == 0) csum[sl * 64 + fbl * 16 + n16] = s;
  }
  __syncthreads();
  if (tid < 64) {
    float a = 0.f;
#pragma unroll
    for (int i = 0; i < 6; ++i) a += csum[i * 64 + tid];
    pst4(cs + (size_t)(k3 * 2 + mat) * F_ + cb * 64 + tid, a);
  }
}
DI void retile_d(int s, const u16* __restrict__ wd, u16* __restrict__ wdt, LAS char* sm) {
  const int tid = threadIdx.x, lane = tid & 63, w = tid >> 6;
  const char* src = (const char*)wd + (size_t)s * 32 * (D_ * 2);
  u32x4 v[5];
#pragma unroll
  for (int i = 0; i < 5; ++i) { const int q = min(tid + 512 * i, 2303); v[i] = NTL((const u32x4*)(src + (size_t)q * 16)); }
#pragma unroll
  for (int i = 0; i < 5; ++i) { const int q = tid + 512 * i, r = q / 72, c = q % 72; if (q < 2304) *(LAS u32x4*)(sm + r * 1152 + (((c & ~7) | ((c & 7) ^ (2 * ((r >> 1) & 3)))) * 16)) = v[i]; }
  __syncthreads();
  const int n16 = lane & 15, g = lane >> 4, q = n16 >> 2, p = lane & 3;
#pragma unroll
  for (int i = 0; i < 5; ++i) {
    const int ct = w + 8 * i;
    if (ct < 36) {
      const int c = 2 * ct + (p >> 1);
      const int r0 = 8 * g + q, r1 = r0 + 4;
      LAS char* a0 = sm + r0 * 1152 + (((c & ~7) | ((c & 7) ^ (2 * ((r0 >> 1) & 3)))) * 16) + 8 * (p & 1);
      LAS char* a1 = sm + r1 * 1152 + (((c & ~7) | ((c & 7) ^ (2 * ((r1 >> 1) & 3)))) * 16) + 8 * (p & 1);
      const u32x4 f = __builtin_bit_cast(u32x4, ld_tr(a0, a1));
      pst16(wdt + ((((size_t)(ct / 9) * 144 + s) * 9 + ct % 9) * 64 + lane) * 8, f);
    }
  }
}
extern "C" __global__ __launch_bounds__(576) void k_r0(const u16* __restrict__ src, u16* __restrict__ xr, float* __restrict__ st, unsigned* __restrict__ cnt,
                                                      const u16* __restrict__ wqkv, u16* __restrict__ wt, float* __restrict__ cwq) {
  __shared__ __attribute__((aligned(16))) char sm[9 * 16 * 4];
  if (blockIdx.x < 256) prep_x(src, xr, st, cnt, (float (*)[8][2])sm);
  else retile_qkv(blockIdx.x - 256, wqkv, wt, cwq, (float (*)[8][2])sm);
}

constexpr int A_STAGE = 16384;
constexpr int A_EX = 4 * A_STAGE;
constexpr int A_LDS = A_EX + 2048;
constexpr int KVR = 344;
DI float phi(float t) { return (t > 0.f ? t + 1.0f : __builtin_amdgcn_exp2f(t * 1.4426950408889634f)) + 1e-6f; }
extern "C" __global__ __launch_bounds__(512, 2) void k_a(const u16* __restrict__ xn, const float* __restrict__ st, const float* __restrict__ cwq, const u16* __restrict__ wt, u16* __restrict__ pq, float* __restrict__ kvp,
                                                        const u16* __restrict__ wo, u16* __restrict__ mt, u16* __restrict__ zt, unsigned* __restrict__ cnt,
                                                        const u16* __restrict__ wg, const u16* __restrict__ wu, u16* __restrict__ wgt, float* __restrict__ cs,
                                                        const u16* __restrict__ wd, u16* __restrict__ wdt) {
  extern __shared__ __attribute__((aligned(16))) char smem_[];
  LAS char* smem = (LAS char*)smem_;
  if (blockIdx.x >= 256) {
    if (blockIdx.x < 688) retile_gu(blockIdx.x - 256, wg, wu, wgt, cs, smem); else retile_d(blockIdx.x - 688, wd, wdt, smem);
    return;
  }
  const int tid = threadIdx.x, lane = tid & 63;
  const int w = __builtin_amdgcn_readfirstlane(tid >> 6);
  const int xcd = blockIdx.x & 7, idx = blockIdx.x >> 3;
  const int tt = (xcd >> 1) * 4 + (idx >> 3), hp = (xcd & 1) * 8 + (idx & 7);
  const unsigned ldsb = (unsigned)(size_t)smem;
  const int g = lane >> 4, t16 = lane & 15;
  const unsigned vlin = lane * 16;
  u32x4 sv0, wv;
  const char* xs = (const char*)xn + (size_t)(tt * 8 + w) * 18432;
  const char* wsrc = (const char*)wt + (size_t)(hp * 7 + min(w, 6)) * 18432;
  auto stage1 = [&](int st, int n) {
    const unsigned dst = ldsb + (st & 3) * A_STAGE + w * 1024;
    if (n == 0) glds16(vlin, xs + st * 1024, dst); else glds16(vlin, wsrc + st * 1024, dst + 8192);
  };
  stage1(0, 0); stage1(0, 1); BAR(); stage1(1, 0); stage1(1, 1); BAR(); stage1(2, 0); stage1(2, 1);
  {
    const int i = min(tid - 256, 55);
    const float* p = tid < 256 ? st + (size_t)(tt * 128) * 8 + tid * 4 : cwq + (size_t)((i / 28) * 112 + hp * 7) * 16 + (i % 28) * 4;
    asm volatile("global_load_dwordx4 %0, %1, off" : "=v"(sv0) : "v"(p) : "memory");
    const u16* pw = wo + (size_t)(36 * hp + min(tid / 6, 35)) * D_ + min(tt, 11) * 48 + (tid % 6) * 8;
    asm volatile("global_load_dwordx4 %0, %1, off" : "=v"(wv) : "v"(pw) : "memory");
  }
  const int tp = w & 3, cgp = w >> 2;
  f32x4 acc[2][4];
#pragma unroll
  for (int m = 0; m < 2; ++m)
#pragma unroll
    for (int j = 0; j < 4; ++j) acc[m][j] = f32x4{0, 0, 0, 0};
  LAS char* xl = smem + lane * 16 + tp * 2048;
  LAS char* wl = smem + lane * 16 + 8192 + cgp * 4096;
  static_for<0, 18>([&](auto ST_) {
    constexpr int st = ST_;
    WAITV(st < 16 ? 4 : st == 16 ? 2 : 0);
    WAITL0(); BAR();
    const int o = (st & 3) * A_STAGE;
    bf16x8 x0 = ld_frag(xl + o), x1 = ld_frag(xl + o + 1024);
    bf16x8 w0 = ld_frag(wl + o), w1 = ld_frag(wl + o + 1024), w2 = ld_frag(wl + o + 2048), w3 = ld_frag(wl + o + (cgp == 0 ? 3072 : 2048));
    SB0();
    if constexpr (st + 3 < 18) stage1(st + 3, 0);
    SB0();
    acc[0][0] = MFMA16(w0, x0, acc[0][0]); acc[1][0] = MFMA16(w0, x1, acc[1][0]);
    acc[0][1] = MFMA16(w1, x0, acc[0][1]); acc[1][1] = MFMA16(w1, x1, acc[1][1]);
    SB0();
    if constexpr (st + 3 < 18) stage1(st + 3, 1);
    SB0();
    acc[0][2] = MFMA16(w2, x0, acc[0][2]); acc[1][2] = MFMA16(w2, x1, acc[1][2]);
    if (cgp == 0) { acc[0][3] = MFMA16(w3, x0, acc[0][3]); acc[1][3] = MFMA16(w3, x1, acc[1][3]); }
    if constexpr (st == 2) {
      asm volatile("" : "+v"(sv0));
      const f32x4 v = __builtin_bit_cast(f32x4, sv0);
      float sm = v[0] + v[2], sq = v[1] + v[3];
      sm += __shfl_xor(sm, 1); sq += __shfl_xor(sq, 1);
      const float mean = sm * (1.0f / D_), var = sq * (1.0f / D_) - mean * mean;
      if (tid < 256) { if ((tid & 1) == 0) *(LAS f32x2*)(smem + A_EX + (tid >> 1) * 8) = f32x2{mean, __builtin_amdgcn_rsqf(fmaxf(var, 0.f) + 1e-5f)}; }
      else if (tid < 312) *(LAS f32x4*)(smem + A_EX + 1024 + (tid - 256) * 16) = v;
    }
    SB0();
  });
  {
#pragma unroll
    for (int m = 0; m < 2; ++m) {
      const f32x2 ms = *(LAS f32x2*)(smem + A_EX + ((2 * tp + m) * 16 + t16) * 8);
#pragma unroll
      for (int j = 0; j < 4; ++j) {
        LAS char* cp = smem + A_EX + 1024 + (min(cgp * 4 + j, 6) * 4 + g) * 16;
        const f32x4 cw = *(LAS f32x4*)cp + *(LAS f32x4*)(cp + 448);
        acc[m][j] = (acc[m][j] - ms[0] * cw) * ms[1];
      }
    }
  }
  WAITL0(); BAR();
  constexpr int KVS = 224;
  if (tid < 128) {
    LAS char* r = smem + tid * KVS;
    *(LAS u32x2*)(r + 72) = u32x2{0, 0}; *(LAS u32x2*)(r + 80) = u32x2{0, 0}; *(LAS u32x2*)(r + 88) = u32x2{0, 0};
    *(LAS u32x2*)(r + 168) = u32x2{0x3f80u, 0}; *(LAS u32x2*)(r + 176) = u32x2{0, 0}; *(LAS u32x2*)(r + 184) = u32x2{0, 0};
  }
#pragma unroll
  for (int m = 0; m < 2; ++m) {
    const int tl = (2 * tp + m) * 16 + t16;
    LAS char* row = smem + tl * KVS;
    const size_t Tg = (size_t)(tt * 8 + 2 * tp + m);
    if (cgp == 0) {
      const f32x4 a = acc[m][0], b = acc[m][1];
      pst16(pq + ((Tg * 18 + hp) * 64 + lane) * 8, u32x4{pk2(phi(a[0]), phi(a[1])), pk2(phi(a[2]), phi(a[3])), pk2(phi(b[0]), phi(b[1])), pk2(phi(b[2]), phi(b[3]))});
      *(LAS u32x2*)(row + 8 * g) = u32x2{pk2(phi(acc[m][2][0]), phi(acc[m][2][1])), pk2(phi(acc[m][2][2]), phi(acc[m][2][3]))};
      *(LAS u32x2*)(row + 32 + 8 * g) = u32x2{pk2(phi(acc[m][3][0]), phi(acc[m][3][1])), pk2(phi(acc[m][3][2]), phi(acc[m][3][3]))};
    } else {
      *(LAS u32x2*)(row + 96 + 8 * g) = u32x2{pk2(acc[m][0][0], acc[m][0][1]), pk2(acc[m][0][2], acc[m][0][3])};
      *(LAS u32x2*)(row + 96 + 32 + 8 * g) = u32x2{pk2(acc[m][1][0], acc[m][1][1]), pk2(acc[m][1][2], acc[m][1][3])};
      const f32x4 a = acc[m][2];
      const u32x2 pv = u32x2{pk2(phi(a[0]), phi(a[1])), pk2(phi(a[2]), phi(a[3]))};
      if (g == 0) pst8(pq + ((Tg * 18 + 16 + (hp >> 3)) * 64 + ((hp >> 1) & 3) * 16 + t16) * 8 + (hp & 1) * 4, pv);
      else if (g == 1) *(LAS u32x2*)(row + 64) = pv;
      else if (g == 2) *(LAS u32x2*)(row + 96 + 64) = u32x2{pk2(a[0], a[1]), pk2(a[2], a[3])};
    }
  }
  WAITL0(); BAR();
  const int kt = (0x02211100 >> (4 * w)) & 3, vt = (0x22121010 >> (4 * w)) & 3;
  f32x4 kv = {0, 0, 0, 0};
  const int q = t16 >> 2, p = lane & 3;
#pragma unroll
  for (int s = 0; s < 4; ++s) {
    LAS char* r0 = smem + (32 * s + 4 * g + q) * KVS + 8 * p;
    bf16x8 a = ld_tr(r0 + kt * 32, r0 + 16 * KVS + kt * 32);
    bf16x8 b = ld_tr(r0 + 96 + vt * 32, r0 + 16 * KVS + 96 + vt * 32);
    kv = MFMA16(a, b, kv);
  }
  {
    LAS float* stg = (LAS float*)(smem + 32768);
    const int e = 16 * vt + t16;
#pragma unroll
    for (int i = 0; i < 4; ++i) {
      const int d = 16 * kt + 4 * g + i;
      if (d < 36) {
        const int hd = d >= 18 ? 1 : 0, dd = d - 18 * hd;
        if (e < 36) { const int he = e >= 18 ? 1 : 0; if (he == hd) stg[hd * 344 + dd * 18 + (e - 18 * he)] = kv[i]; }
        else if (e == 36) stg[hd * 344 + 324 + dd] = kv[i];
      }
    }
    if (tid < 4) stg[(tid >> 1) * 344 + 342 + (tid & 1)] = 0.f;
    WAITL0(); BAR();
    if (tid < 172) pst16(kvp + ((size_t)tt * 32 + 2 * hp) * 344 + tid * 4, *(LAS u32x4*)(stg + tid * 4));
  }
  asm volatile("s_waitcnt vmcnt(0)" : "+v"(wv) :: "memory");
  BAR();
  unsigned* cw = cnt + hp * 32;
  if (tid == 0) __hip_atomic_fetch_add(cw, 1u, __ATOMIC_RELAXED, __HIP_MEMORY_SCOPE_AGENT);
  if (tt < 12) {
    const int cgp = tt;
    LAS float* kvs = (LAS float*)(smem + 36864);
    LAS char* wos = smem + 36864 + 2 * KVR * 4;
    if (tid < 384) *(LAS u32x4*)(wos + (tid / 6) * 112 + (tid % 6) * 16) = tid < 216 ? wv : u32x4{0, 0, 0, 0};
    if (w == 0) {
      for (int spin = 0; spin < (1 << 16); ++spin) {
        unsigned v;
        asm volatile("global_load_dword %0, %1, %2 sc1\n\ts_waitcnt vmcnt(0)" : "=v"(v) : "v"(0u), "s"(cw) : "memory");
        if (__builtin_amdgcn_readfirstlane(v) >= 16u) break;
        __builtin_amdgcn_s_sleep(1);
      }
    }
    BAR();
    if (w < 3) {
      const char* src = (const char*)(kvp + (size_t)2 * hp * KVR);
      const unsigned vo = min(tid, 171) * 16;
      u32x4 v[16];
#pragma unroll
      for (int p2 = 0; p2 < 16; ++p2) asm volatile("global_load_dwordx4 %0, %1, %2 sc1" : "=v"(v[p2]) : "v"(vo), "s"(src + (size_t)p2 * 32 * KVR * 4) : "memory");
      asm volatile("s_waitcnt vmcnt(0)" : "+v"(v[0]), "+v"(v[1]), "+v"(v[2]), "+v"(v[3]), "+v"(v[4]), "+v"(v[5]), "+v"(v[6]), "+v"(v[7]),
                   "+v"(v[8]), "+v"(v[9]), "+v"(v[10]), "+v"(v[11]), "+v"(v[12]), "+v"(v[13]), "+v"(v[14]), "+v"(v[15]) :: "memory");
      f32x4 f[16];
#pragma unroll
      for (int p2 = 0; p2 < 16; ++p2) f[p2] = __builtin_bit_cast(f32x4, v[p2]);
      f32x4 a = ((f[0] + f[1]) + (f[2] + f[3])) + ((f[4] + f[5]) + (f[6] + f[7])) + (((f[8] + f[9]) + (f[10] + f[11])) + ((f[12] + f[13]) + (f[14] + f[15])));
#pragma unroll
      for (int i = 0; i < 4; ++i) a[i] = bf2f(f2bf(a[i]));
      if (tid < 172) *(LAS f32x4*)(kvs + tid * 4) = a;
    }
    WAITL0(); BAR();
    const int n16 = t16;
    if (w < 6) {
      const int ct = w % 3;
      auto rowtile = [&](int rt) -> u32x2 {
        const int c = min(16 * rt + n16, 35), hh = c >= 18 ? 1 : 0, d = c - 18 * hh;
        const LAS float* kr = kvs + hh * KVR + d * 18;
        const bool rowok = 16 * rt + n16 < 36;
        f32x4 acc2 = {0, 0, 0, 0};
#pragma unroll
        for (int u = 0; u < 2; ++u) {
          float av[8];
#pragma unroll
          for (int j = 0; j < 8; ++j) { const int e = 32 * u + 8 * g + j - 18 * hh; const bool ok = rowok && e >= 0 && e < 18; const float x = kr[ok ? e : 0]; av[j] = ok ? x : 0.f; }
          const u32x4 af = {pk2(av[0], av[1]), pk2(av[2], av[3]), pk2(av[4], av[5]), pk2(av[6], av[7])};
          LAS char* wb2 = wos + (32 * u + 8 * g + q) * 112 + (ct * 16 + 4 * p) * 2;
          const bf16x8 bf = ld_tr(wb2, wb2 + 4 * 112);
          acc2 = MFMA16(__builtin_bit_cast(bf16x8, af), bf, acc2);
        }
        return u32x2{pk2(acc2[0], acc2[1]), pk2(acc2[2], acc2[3])};
      };
      const int jglob = cgp * 3 + ct;
      if (w < 3) { const u32x2 a = rowtile(0), b = rowtile(1); pst16(mt + (((size_t)jglob * 18 + hp) * 64 + lane) * 8, u32x4{a[0], a[1], b[0], b[1]}); }
      else { const u32x2 o = rowtile(2); if (g == 0) pst8(mt + (((size_t)jglob * 18 + 16 + (hp >> 3)) * 64 + ((hp >> 1) & 3) * 16 + n16) * 8 + (hp & 1) * 4, o); }
    } else if (cgp == 0 && w == 7) {
      const int hrow = (hp >> 3) * 16 + n16;
      float o[8];
#pragma unroll
      for (int e8 = 0; e8 < 8; ++e8) {
        const int c = 16 * (e8 >> 2) + 4 * g + (e8 & 3), hh = c >= 18 ? 1 : 0;
        o[e8] = (2 * hp + hh == hrow) ? kvs[hh * KVR + 324 + c - 18 * hh] : 0.f;
      }
      pst16(zt + ((size_t)hp * 64 + lane) * 8, u32x4{pk2(o[0], o[1]), pk2(o[2], o[3]), pk2(o[4], o[5]), pk2(o[6], o[7])});
      if (g == 0) {
        const bool mine = (2 * hp + 1 == hrow);
        float z4[4];
#pragma unroll
        for (int i = 0; i < 4; ++i) z4[i] = mine ? kvs[KVR + 324 + 14 + i] : 0.f;
        pst8(zt + (((size_t)16 + (hp >> 3)) * 64 + ((hp >> 1) & 3) * 16 + n16) * 8 + (hp & 1) * 4, u32x2{pk2(z4[0], z4[1]), pk2(z4[2], z4[3])});
      }
    }
  }
}

extern "C" __global__ __launch_bounds__(576) void k_b(const u16* __restrict__ pq, const u16* __restrict__ mt, const u16* __restrict__ zt, const u16* __restrict__ xr,
                                           u16* __restrict__ x1, float* __restrict__ st) {
  extern __shared__ __attribute__((aligned(16))) char smem_[];
  LAS char* smem = (LAS char*)smem_;
  constexpr int SQ = 0, SZ = 36864, SR = SZ + 18432, SS = SR + 32 * 33 * 4;
  const int tid = threadIdx.x, lane = tid & 63;
  const int w = __builtin_amdgcn_readfirstlane(tid >> 6);
  const int xcd = blockIdx.x & 7, idx = blockIdx.x >> 3;
  const int tt = xcd * 8 + (idx >> 2), nt = idx & 3;
  const int g = lane >> 4, t16 = lane & 15;
  const unsigned ldsb = (unsigned)(size_t)smem, vlin = lane * 16;
#pragma unroll
  for (int i = 0; i < 4; ++i) glds16(vlin, (const char*)pq + ((size_t)tt * 36 + w * 4 + i) * 1024, ldsb + SQ + (w * 4 + i) * 1024);
#pragma unroll
  for (int i = 0; i < 2; ++i) glds16(vlin, (const char*)zt + (w * 2 + i) * 1024, ldsb + SZ + (w * 2 + i) * 1024);
  u32x4 mf[18];
  const char* mb = (const char*)mt + (size_t)(nt * 9 + w) * 18432;
  auto ldm = [&](int s) { asm volatile("global_load_dwordx4 %0, %1, %2" : "=v"(mf[s]) : "v"(vlin), "s"(mb + s * 1024) : "memory"); };
  ldm(0); ldm(1); ldm(2);
  WAITV(3);
  BAR();
  {
    f32x4 d = {0, 0, 0, 0};
    bf16x8 za[9], pb[9];
    const int wd_ = w & 3;
#pragma unroll
    for (int s = 0; s < 9; ++s) {
      const int ks = s < 8 ? (wd_ & 1) * 8 + s : 16 + (wd_ & 1);
      if (w < 4) { za[s] = ld_frag(smem + SZ + ks * 1024 + vlin); pb[s] = ld_frag(smem + SQ + ((wd_ >> 1) * 18 + ks) * 1024 + vlin); }
    }
    SB0();
    ldm(3); ldm(4);
    SB0();
    if (w < 4) {
#pragma unroll
      for (int s = 0; s < 9; ++s) d = MFMA16(za[s], pb[s], d);
    }
    SB0();
    ldm(5);
    SB0();
    if (w < 4) {
      LAS float* rd = (LAS float*)(smem + SR) + ((w >> 1) * 16 + t16) * 33 + (w & 1) * 16 + 4 * g;
#pragma unroll
      for (int i = 0; i < 4; ++i) rd[i] = __builtin_amdgcn_rcpf(d[i] + 1e-6f);
    }
    SB0();
    ldm(6); ldm(7);
  }
  WAITL0(); BAR();
  {
    u32x4 v[4]; float r[4][3];
#pragma unroll
    for (int i = 0; i < 4; ++i) {
      const int f = w * 4 + i, T = f >= 18 ? 1 : 0, s = f - 18 * T;
      const bool lo = s >= 16;
      const int qq = 16 * (s - 16) + 4 * g;
      const int ha = lo ? qq + 1 : 2 * s, hc = lo ? qq + 3 : 2 * s + (g > 0 ? 1 : 0), hd = lo ? qq + 3 : 2 * s + 1;
      const LAS float* rd = (const LAS float*)(smem + SR) + (T * 16 + t16) * 33;
      v[i] = *(LAS u32x4*)(smem + SQ + f * 1024 + vlin);
      r[i][0] = rd[ha]; r[i][1] = rd[hc]; r[i][2] = rd[hd];
    }
    SB0();
    ldm(8);
    SB0();
#pragma unroll
    for (int i = 0; i < 4; ++i) {
      *(LAS u32x4*)(smem + SQ + (w * 4 + i) * 1024 + vlin) = u32x4{pk2(lo16(v[i][0]) * r[i][0], hi16(v[i][0]) * r[i][0]), pk2(lo16(v[i][1]) * r[i][0], hi16(v[i][1]) * r[i][0]),
                                                                  pk2(lo16(v[i][2]) * r[i][1], hi16(v[i][2]) * r[i][1]), pk2(lo16(v[i][3]) * r[i][2], hi16(v[i][3]) * r[i][2])};
      SB0();
      ldm(9 + i);
      SB0();
    }
  }
  WAITL0(); BAR();
  u32x2 xres[2];
  const int cb = nt * 9 + w;
  const char* xrb = (const char*)xr + ((size_t)(tt * 2) * 18 + (cb >> 1)) * 1024 + (cb & 1) * 8;
  f32x4 acc[2] = {{0, 0, 0, 0}, {0, 0, 0, 0}};
  asm volatile("s_waitcnt vmcnt(5)" : "+v"(mf[0]), "+v"(mf[1]), "+v"(mf[2]), "+v"(mf[3]), "+v"(mf[4]), "+v"(mf[5]), "+v"(mf[6]), "+v"(mf[7]) :: "memory");
  static_for<0, 18>([&](auto S_) {
    constexpr int s = S_;
    if constexpr (s < 5) ldm(13 + s);
    if constexpr (s == 5) { asm volatile("global_load_dwordx2 %0, %1, %2" : "=v"(xres[0]) : "v"(vlin), "s"(xrb) : "memory"); asm volatile("global_load_dwordx2 %0, %1, %2" : "=v"(xres[1]) : "v"(vlin), "s"(xrb + 18432) : "memory"); }
    if constexpr (s == 8) asm volatile("s_waitcnt vmcnt(7)" : "+v"(mf[8]), "+v"(mf[9]), "+v"(mf[10]), "+v"(mf[11]), "+v"(mf[12]) :: "memory");
    if constexpr (s == 13) asm volatile("s_waitcnt vmcnt(2)" : "+v"(mf[13]), "+v"(mf[14]), "+v"(mf[15]), "+v"(mf[16]), "+v"(mf[17]) :: "memory");
    const bf16x8 b0 = ld_frag(smem + SQ + s * 1024 + vlin), b1 = ld_frag(smem + SQ + (18 + s) * 1024 + vlin);
    acc[0] = MFMA16(__builtin_bit_cast(bf16x8, mf[s]), b0, acc[0]);
    acc[1] = MFMA16(__builtin_bit_cast(bf16x8, mf[s]), b1, acc[1]);
    SB0();
  });
  asm volatile("s_waitcnt vmcnt(0)" : "+v"(xres[0]), "+v"(xres[1]) :: "memory");
  LAS float* sred = (LAS float*)(smem + SS);
  u32x2 ox[2];
#pragma unroll
  for (int m = 0; m < 2; ++m) {
    const unsigned o0 = pk2(acc[m][0] + lo16(xres[m][0]), acc[m][1] + hi16(xres[m][0])), o1 = pk2(acc[m][2] + lo16(xres[m][1]), acc[m][3] + hi16(xres[m][1]));
    ox[m] = u32x2{o0, o1};
    const float a0 = lo16(o0), a1 = hi16(o0), a2 = lo16(o1), a3 = hi16(o1);
    float sm = (a0 + a1) + (a2 + a3), sq2 = (a0 * a0 + a1 * a1) + (a2 * a2 + a3 * a3);
    sm += __shfl_xor(sm, 16); sq2 += __shfl_xor(sq2, 16);
    sm += __shfl_xor(sm, 32); sq2 += __shfl_xor(sq2, 32);
    if (g == 0) { sred[(w * 32 + m * 16 + t16) * 2] = sm; sred[(w * 32 + m * 16 + t16) * 2 + 1] = sq2; }
  }
  {
    auto r0 = __builtin_amdgcn_permlane32_swap(ox[0][0], ox[1][0], false, false);
    auto r1 = __builtin_amdgcn_permlane32_swap(ox[0][1], ox[1][1], false, false);
    auto q0 = __builtin_amdgcn_permlane16_swap(r0[0], r0[1], false, false);
    auto q1 = __builtin_amdgcn_permlane16_swap(r1[0], r1[1], false, false);
    pst16((char*)x1 + ((size_t)(tt * 2 + (lane >> 5)) * 18 + (cb >> 1)) * 1024 + (cb & 1) * 512 + ((lane >> 4) & 1) * 256 + t16 * 16, u32x4{q0[0], q1[0], q0[1], q1[1]});
  }
  WAITL0(); BAR();
  if (tid < 64) {
    const int t = tid >> 1, c = tid & 1;
    float a = 0.f;
#pragma unroll
    for (int i = 0; i < 9; ++i) a += sred[(i * 32 + t) * 2 + c];
    pst4(st + ((size_t)(tt * 32 + t) * 4 + nt) * 2 + c, a);
  }
}
constexpr int B_LDS = 36864 + 18432 + 32 * 33 * 4 + 9 * 32 * 2 * 4;

extern "C" void kernel_launch(void* const* d_in, const int* in_sizes, int n_in, void* d_out, int out_size, void* d_ws, size_t ws_size, hipStream_t stream) {
  const u16* x = (const u16*)d_in[0]; const u16* wqkv = (const u16*)d_in[1]; const u16* wo = (const u16*)d_in[2];
  const u16* wg = (const u16*)d_in[4]; const u16* wu = (const u16*)d_in[5]; const u16* wd = (const u16*)d_in[6];
  char* ws = (char*)d_ws; size_t off = 0; auto al = [&](size_t b) { void* p = ws + off; off += (b + 1023) / 1024 * 1024; return p; };
  unsigned* cnt = (unsigned*)al(160 * 32 * 4);
  u16* xr = (u16*)al((size_t)S_ * D_ * 2); float* st0 = (float*)al((size_t)S_ * 8 * 4); u16* wt = (u16*)al((size_t)2016 * 1024); float* cwq = (float*)al(2 * 112 * 16 * 4);
  u16* pq = (u16*)al((size_t)S_ * D_ * 2); float* kvp = (float*)al((size_t)16 * 32 * 344 * 4); u16* mt = (u16*)al((size_t)36 * 18 * 1024); u16* zt = (u16*)al(18 * 1024);
  u16* x1 = (u16*)al((size_t)S_ * D_ * 2); float* st = (float*)al((size_t)S_ * 8 * 4); u16* h = (u16*)al((size_t)S_ * F_ * 2); u16* ex = (u16*)al((size_t)64 * 16 * 9216);
  u16* wgt = (u16*)al((size_t)2 * D_ * F_ * 2); u16* wdt = (u16*)al((size_t)D_ * F_ * 2); float* cs = (float*)al((size_t)6 * F_ * 4);
  (void)hipFuncSetAttribute((const void*)k_a, hipFuncAttributeMaxDynamicSharedMemorySize, A_LDS);
  (void)hipFuncSetAttribute((const void*)k_c, hipFuncAttributeMaxDynamicSharedMemorySize, C_LDS);
  (void)hipFuncSetAttribute((const void*)k_d, hipFuncAttributeMaxDynamicSharedMemorySize, D_LDS);
  k_r0<<<256 + 224, 576, 0, stream>>>(x, xr, st0, cnt, wqkv, wt, cwq);
  for (int l = 0; l < 2; ++l) {
    k_a<<<256 + (l == 0 ? 432 + 144 : 0), 512, A_LDS, stream>>>(xr, st0, cwq, wt, pq, kvp, wo, mt, zt, cnt + (128 + l * 16) * 32, wg, wu, wgt, cs, wd, wdt);
    k_b<<<256, 576, B_LDS, stream>>>(pq, mt, zt, xr, x1, st);
    k_c<<<256, 512, C_LDS, stream>>>(x1, st, wgt, cs, h);
    k_d<<<256, 512, D_LDS, stream>>>(h, wdt, x1, ex, cnt + l * 64 * 32, xr, st0, l == 0 ? nullptr : (u16*)d_out);
  }
}
```
